```python
import torch

def _softmax(x, axis=-1):
    return torch.softmax(x, dim=axis)
def _gelu(x):
    return torch.nn.functional.gelu(x, approximate="tanh")
def _silu(x):
    return torch.nn.functional.silu(x)
def _sigmoid(x):
    return torch.sigmoid(x)
def _layernorm(x, axis=-1, eps=1e-5):
    m = x.mean(dim=axis, keepdim=True)
    v = x.var(dim=axis, keepdim=True, unbiased=False)
    return (x - m) / torch.sqrt(v + eps)
def _scatter_add(tgt, idx, src, axis=0):
    return tgt.clone().index_add_(axis, idx, src)
def _segment_sum(src, seg, n):
    out = torch.zeros((n,) + src.shape[1:], dtype=src.dtype)
    return out.index_add_(0, seg, src)
def _pad(x, pw):
    flat = tuple(v for pair in reversed(pw) for v in pair)
    return torch.nn.functional.pad(x, flat)

kernel_name = 'recurrent_scan__ssm_conv_s4d__mixer_channel_mlp__prompt'

def setup_inputs():

    _w = (lambda a: (lambda t: t.to(torch.bfloat16) if t.is_floating_point() else t)(torch.as_tensor(a)))
    def _A_setup():
        Bsz, L, D, N = 1, 2048, 448, 256
        return {
            "u": _w((lambda *s: torch.randn(*s, dtype=torch.bfloat16) * 0.05)(Bsz, L, D)),
            "log_A_real": _w((lambda *s: torch.randn(*s, dtype=torch.bfloat16) * 3.2)(D, N) * 0.5),
            "A_imag": _w((lambda *s: torch.randn(*s, dtype=torch.bfloat16) * 0.2)(D, N) * 3.14),
            "C_real": _w((lambda *s: torch.randn(*s, dtype=torch.bfloat16) * 0.8)(D, N) / N ** 0.5),
            "C_imag": _w((lambda *s: torch.randn(*s, dtype=torch.bfloat16) * 0.8)(D, N) / N ** 0.5),
            "log_dt": _w((lambda *s: torch.randn(*s, dtype=torch.bfloat16) * 0.2)(D) * 0.5 - 4.0),
            "D_skip": _w((lambda *s: torch.randn(*s, dtype=torch.bfloat16) * 0.2)(D)),
        }
    _A_in = _A_setup()

    def _B_setup():
        B, S, D, F = 1, 2048, 448, 1792
        return {
            "x": _w((lambda *s: torch.randn(*s, dtype=torch.bfloat16) * 0.05)(B, S, D)),
            "w1": _w((lambda *s: torch.randn(*s, dtype=torch.bfloat16) * 1.6)(D, F) * (D ** -0.5)),
            "b1": _w((lambda *s: torch.randn(*s, dtype=torch.bfloat16) * 0.8)(F)),
            "w2": _w((lambda *s: torch.randn(*s, dtype=torch.bfloat16) * 0.1)(F, D) * (F ** -0.5)),
            "b2": _w((lambda *s: torch.randn(*s, dtype=torch.bfloat16) * 0.1)(D)),
        }
    _B_in = _B_setup()

    return {
        "A_u": _A_in["u"],
        "A_log_A_real": _A_in["log_A_real"],
        "A_A_imag": _A_in["A_imag"],
        "A_C_real": _A_in["C_real"],
        "A_C_imag": _A_in["C_imag"],
        "A_log_dt": _A_in["log_dt"],
        "A_D_skip": _A_in["D_skip"],
        "B_x": _B_in["x"],
        "B_w1": _B_in["w1"],
        "B_b1": _B_in["b1"],
        "B_w2": _B_in["w2"],
        "B_b2": _B_in["b2"],
    }

def reference(A_u, A_log_A_real, A_A_imag, A_C_real, A_C_imag, A_log_dt, A_D_skip, B_x, B_w1, B_b1, B_w2, B_b2):
    def _A_ref(u, log_A_real, A_imag, C_real, C_imag, log_dt, D_skip):
        L = u.shape[1]
        dt = torch.exp(log_dt)
        A = -torch.exp(log_A_real) + 1j * A_imag
        C = (C_real + 1j * C_imag) * dt[:, None]
        pos = (lambda x, dt: x.to(dt))(torch.arange(L), log_dt.dtype)
        P = torch.exp((A * dt[:, None])[:, :, None] * pos[None, None, :])
        K = 2.0 * torch.einsum("dn,dnl->dl", C, P).real
        L2 = 2 * L
        u_t = (lambda x, *d: x.permute(*d))(u, 0, 2, 1)
        Kf = (lambda x, *a, **k: torch.fft.rfft(x.float(), *a, **k))(K, L2)
        Uf = (lambda x, *a, **k: torch.fft.rfft(x.float(), *a, **k))(u_t, L2)
        y = (lambda x, *a, **k: torch.fft.irfft(x, *a, **k).to(torch.bfloat16))(Uf * Kf[None, :, :], L2)[:, :, :L]
        y = (lambda x, *d: x.permute(*d))(y, 0, 2, 1)
        return y + u * D_skip[None, None, :]

    def _B_ref(x, w1, b1, w2, b2):
        y = _layernorm(x, axis=-1)
        h = _gelu(torch.einsum("bsd,df->bsf", y, w1) + b1)
        return torch.einsum("bsf,fd->bsd", h, w2) + b2

    x = A_u
    xn = _layernorm(x, axis=-1)
    s = _A_ref(xn, A_log_A_real, A_A_imag, A_C_real, A_C_imag, A_log_dt, A_D_skip)
    g = _silu(_B_ref(xn, B_w1, B_b1, B_w2, B_b2))
    return x + s * g
```

```cpp
#include <hip/hip_runtime.h>
#include <hip/hip_bf16.h>
#define POS_BF16 1
typedef unsigned short u16;
__device__ __forceinline__ float bf2f(u16 b){ return __builtin_bit_cast(float,(unsigned)b<<16);}
__device__ __forceinline__ u16 f2bf(float f){ unsigned u=__builtin_bit_cast(unsigned,f); u+=0x7FFFu+((u>>16)&1u); return (u16)(u>>16);}
__device__ __forceinline__ float rbf(float f){ return bf2f(f2bf(f)); }
constexpr int L=2048,D=448,N=256,F=1792;

__global__ void k_ln(const u16* x,u16* xn,u16* y2){
  int t=blockIdx.x; int lane=threadIdx.x;
  float v[7]; float s=0;
  for(int i=0;i<7;i++){ v[i]=bf2f(x[t*D+lane+64*i]); s+=v[i]; }
  for(int pass=0;pass<2;pass++){
    s=0; for(int i=0;i<7;i++) s+=v[i];
    for(int o=32;o;o>>=1) s+=__shfl_xor(s,o);
    float m=s/D; float q=0; for(int i=0;i<7;i++) q+=(v[i]-m)*(v[i]-m);
    for(int o=32;o;o>>=1) q+=__shfl_xor(q,o);
    float var=rbf(q/D); float mb=rbf(m);
    float sd=rbf(sqrtf(rbf(var+1e-5f)));
    for(int i=0;i<7;i++){ v[i]=rbf(rbf(v[i]-mb)/sd); (pass?y2:xn)[t*D+lane+64*i]=f2bf(v[i]); }
  }
}
__global__ void k_kgen(const u16* lA,const u16* Ai,const u16* Cr,const u16* Ci,const u16* ldt,float* K){
  int d=blockIdx.y; int l=blockIdx.x*256+threadIdx.x;
  __shared__ float ar[N],ai[N],cr[N],ci[N];
  float dt=rbf(expf(bf2f(ldt[d])));
  { int n=threadIdx.x; ar[n]=-rbf(expf(bf2f(lA[d*N+n])))*dt; ai[n]=bf2f(Ai[d*N+n])*dt; cr[n]=bf2f(Cr[d*N+n])*dt; ci[n]=bf2f(Ci[d*N+n])*dt; }
  __syncthreads();
  float p=POS_BF16? rbf((float)l):(float)l;
  float s=0;
  for(int n=0;n<N;n++){ float e=expf(ar[n]*p); float sn,cs; sincosf(ai[n]*p,&sn,&cs); s+=e*(cr[n]*cs-ci[n]*sn); }
  K[l*D+d]=2.f*s;
}
__global__ void k_conv(const float* K,const u16* xn,const u16* Dk,u16* sOut){
  int l=blockIdx.x; int d=threadIdx.x; if(d>=D) return;
  float s=0;
  for(int j=0;j<=l;j++) s+=K[j*D+d]*bf2f(xn[(l-j)*D+d]);
  float y=rbf(s); float u=bf2f(xn[l*D+d]);
  sOut[l*D+d]=f2bf(y+rbf(u*bf2f(Dk[d])));
}
__global__ void k_g1(const u16* y2,const u16* w1,const u16* b1,u16* h){
  int t=blockIdx.y; int f=blockIdx.x*256+threadIdx.x;
  __shared__ float a[D];
  for(int i=threadIdx.x;i<D;i+=256) a[i]=bf2f(y2[t*D+i]);
  __syncthreads();
  float s=0; for(int k=0;k<D;k++) s+=a[k]*bf2f(w1[k*F+f]);
  float x=rbf(rbf(s)+bf2f(b1[f]));
  float g=0.5f*x*(1.f+tanhf(0.7978845608f*(x+0.044715f*x*x*x)));
  h[t*F+f]=f2bf(g);
}
__global__ void k_g2(const u16* h,const u16* w2,const u16* b2,const u16* x,const u16* sIn,u16* out){
  int t=blockIdx.x; int d=threadIdx.x;
  __shared__ float a[F];
  for(int i=threadIdx.x;i<F;i+=blockDim.x) a[i]=bf2f(h[t*F+i]);
  __syncthreads();
  if(d>=D) return;
  float s=0; for(int k=0;k<F;k++) s+=a[k]*bf2f(w2[k*D+d]);
  float o=rbf(rbf(s)+bf2f(b2[d]));
  float g=rbf(o/(1.f+expf(-o)));
  out[t*D+d]=f2bf(bf2f(x[t*D+d])+rbf(bf2f(sIn[t*D+d])*g));
}
extern "C" void kernel_launch(void* const* d_in, const int* in_sizes, int n_in, void* d_out, int out_size, void* d_ws, size_t ws_size, hipStream_t stream){
  const u16* x=(const u16*)d_in[0];
  char* ws=(char*)d_ws;
  u16* xn=(u16*)ws; u16* y2=(u16*)(ws+(2<<20)); u16* sB=(u16*)(ws+(4<<20)); float* K=(float*)(ws+(6<<20)); u16* h=(u16*)(ws+(12<<20));
  k_ln<<<L,64,0,stream>>>(x,xn,y2);
  k_kgen<<<dim3(L/256,D),256,0,stream>>>((const u16*)d_in[1],(const u16*)d_in[2],(const u16*)d_in[3],(const u16*)d_in[4],(const u16*)d_in[5],K);
  k_conv<<<L,448,0,stream>>>(K,xn,(const u16*)d_in[6],sB);
  k_g1<<<dim3(F/256,L),256,0,stream>>>(y2,(const u16*)d_in[8],(const u16*)d_in[9],h);
  k_g2<<<L,448,0,stream>>>(h,(const u16*)d_in[10],(const u16*)d_in[11],x,sB,(u16*)d_out);
}
```

```cpp
#include <hip/hip_runtime.h>
#include <hip/hip_bf16.h>

typedef unsigned short u16;
typedef unsigned long long u64;
typedef unsigned v4u __attribute__((ext_vector_type(4)));
typedef unsigned v2u __attribute__((ext_vector_type(2)));
typedef float f2 __attribute__((ext_vector_type(2)));
typedef float f4 __attribute__((ext_vector_type(4)));
typedef float f16v __attribute__((ext_vector_type(16)));
typedef short s4 __attribute__((ext_vector_type(4)));
typedef __bf16 b8 __attribute__((ext_vector_type(8)));
typedef __bf16 b2 __attribute__((ext_vector_type(2)));
typedef _Float16 h8 __attribute__((ext_vector_type(8)));
typedef _Float16 h2 __attribute__((ext_vector_type(2)));
typedef __amdgpu_buffer_rsrc_t rsrc_t;
#define LAS __attribute__((address_space(3)))
#define CAS __attribute__((address_space(4)))
#define DEV __device__ __forceinline__
#define SB() __builtin_amdgcn_sched_barrier(0)
#define VMW(n) asm volatile("s_waitcnt vmcnt(" #n ")" ::: "memory")
#define LGK0() asm volatile("s_waitcnt lgkmcnt(0)" ::: "memory")
#define BAR() do { asm volatile("s_waitcnt lgkmcnt(0)\n\ts_barrier" ::: "memory"); } while (0)

#define STAMPS 0
#define STAMP(i) do {} while (0)

constexpr int LT = 2048, DM = 448, FF = 1792;
constexpr unsigned WS_E = 0, WS_FX = 1024, WS_FH = 4096, WS_FS = 32768, WS_ST = 65536, WS_XN = 131072,
                   WS_S = WS_XN + 224 * 8192, WS_H = WS_S + 224 * 8192, WS_END = WS_H + 64 * 112 * 1024, WS_DBG = 16u << 20;
constexpr unsigned SPARE = 131072, KREV = SPARE + 12288, KCP = 4224, UARR = 65536, UCP = 4640, YCH = 9216, PROW = 272;
constexpr unsigned CODE_PF_BYTES = 27648;
constexpr unsigned SPIN_CAP = 1u << 20;

struct Params {
  const u16 *x, *logA, *Aim, *Cr, *Ci, *ldt, *Dsk, *w1, *b1, *w2, *b2;
  u16* out;
  char* ws;
};

DEV rsrc_t mkrs(const void* p, unsigned bytes) { return __builtin_amdgcn_make_buffer_rsrc((void*)p, (short)0, (int)bytes, 0x00020000); }
DEV float asf(unsigned u) { return __builtin_bit_cast(float, u); }
DEV unsigned asu(float f) { return __builtin_bit_cast(unsigned, f); }
DEV float bflo(unsigned u) { return asf(u << 16); }
DEV float bfhi(unsigned u) { return asf(u & 0xffff0000u); }
DEV unsigned pkbf(float a, float b) { f2 v = {a, b}; return __builtin_bit_cast(unsigned, __builtin_convertvector(v, b2)); }
DEV unsigned pkh(float a, float b) { f2 v = {a, b}; return __builtin_bit_cast(unsigned, __builtin_convertvector(v, h2)); }
DEV float rbf(float a) { return bflo(pkbf(a, a)); }
DEV float hlo(unsigned u) { return (float)__builtin_bit_cast(h2, u).x; }
DEV float hhi(unsigned u) { return (float)__builtin_bit_cast(h2, u).y; }
template <typename T> DEV LAS T* lp(unsigned a) { return (LAS T*)(size_t)a; }
DEV void dma16(rsrc_t rs, unsigned voff, unsigned soff, unsigned lds) {
  asm volatile("s_mov_b32 m0, %0\n\ts_nop 0\n\tbuffer_load_dwordx4 %1, %2, %3 offen lds" :: "s"(lds), "v"(voff), "s"(rs), "s"(soff) : "memory");
}
#define ALD_U16(d, vo, rs, so) asm volatile("buffer_load_ushort %0, %1, %2, %3 offen" : "=v"(d) : "v"(vo), "s"(rs), "s"(so) : "memory")
#define ALD_B32(d, vo, rs, so) asm volatile("buffer_load_dword %0, %1, %2, %3 offen" : "=v"(d) : "v"(vo), "s"(rs), "s"(so) : "memory")
#define ALD_B64S(d, vo, rs, so) asm volatile("buffer_load_dwordx2 %0, %1, %2, %3 offen sc1" : "=v"(d) : "v"(vo), "s"(rs), "s"(so) : "memory")
#define ALD_B128S(d, vo, rs, so) asm volatile("buffer_load_dwordx4 %0, %1, %2, %3 offen sc1" : "=v"(d) : "v"(vo), "s"(rs), "s"(so) : "memory")
DEV void st128s(v4u v, rsrc_t rs, unsigned vo, unsigned so) { __builtin_amdgcn_raw_buffer_store_b128(v, rs, vo + so, 0, 16); }
DEV float dot2one(unsigned x, float s) { const b2 ones = {(__bf16)1.0f, (__bf16)1.0f}; return __builtin_amdgcn_fdot2_f32_bf16(__builtin_bit_cast(b2, x), ones, s, false); }
DEV void st64s(v2u v, rsrc_t rs, unsigned vo, unsigned so) { __builtin_amdgcn_raw_buffer_store_b64(v, rs, vo, so, 16); }
DEV v4u trfrag(unsigned a0, unsigned a1) {
  s4 lo = __builtin_amdgcn_ds_read_tr16_b64_v4i16(lp<s4>(a0)), hi = __builtin_amdgcn_ds_read_tr16_b64_v4i16(lp<s4>(a1));
  v2u l = __builtin_bit_cast(v2u, lo), h = __builtin_bit_cast(v2u, hi);
  return v4u{l.x, l.y, h.x, h.y};
}
DEV f16v mfma32(v4u a, v4u b, f16v c) { return __builtin_amdgcn_mfma_f32_32x32x16_bf16(__builtin_bit_cast(b8, a), __builtin_bit_cast(b8, b), c, 0, 0, 0); }
DEV f4 mfma16h(v4u a, v4u b, f4 c) { return __builtin_amdgcn_mfma_f32_16x16x32_f16(__builtin_bit_cast(h8, a), __builtin_bit_cast(h8, b), c, 0, 0, 0); }
template <int C, int RM> DEV float dppf(float v) { return asf(__builtin_amdgcn_update_dpp(0, asu(v), C, RM, 0xf, true)); }
DEV float wave_sum(float v) {
  v += dppf<0xB1, 0xf>(v); v += dppf<0x4E, 0xf>(v); v += dppf<0x141, 0xf>(v); v += dppf<0x140, 0xf>(v);
  v += dppf<0x142, 0xa>(v); v += dppf<0x143, 0xc>(v);
  return asf(__builtin_amdgcn_readlane(asu(v), 63));
}
template <int C, int RM> DEV unsigned dppu(unsigned v) { return __builtin_amdgcn_update_dpp(0, v, C, RM, 0xf, true); }
DEV unsigned wave_maxu(unsigned v) {
  v = max(v, dppu<0xB1, 0xf>(v)); v = max(v, dppu<0x4E, 0xf>(v)); v = max(v, dppu<0x141, 0xf>(v)); v = max(v, dppu<0x140, 0xf>(v));
  v = max(v, dppu<0x142, 0xa>(v)); v = max(v, dppu<0x143, 0xc>(v));
  return __builtin_amdgcn_readlane(v, 63);
}
DEV void spin64(v2u f, bool need, rsrc_t rs, unsigned vo, unsigned tlo, unsigned thi) {
#pragma unroll 1
  for (unsigned n = 0; n < SPIN_CAP; ++n) {
    if (__all(!need || (f.x == tlo && f.y == thi))) break;
    __builtin_amdgcn_s_sleep(2);
    asm volatile("buffer_load_dwordx2 %0, %1, %2, 0 offen sc1\n\ts_waitcnt vmcnt(0)" : "=&v"(f) : "v"(vo), "s"(rs) : "memory");
  }
}
struct c32 { float r, i; };
DEV c32 cmul(c32 a, c32 b) { return c32{__builtin_fmaf(a.r, b.r, -a.i * b.i), __builtin_fmaf(a.r, b.i, a.i * b.r)}; }

template <int TH> DEV void conv_loop(unsigned fa, unsigned ga, f4 (&acc)[4]) {
  constexpr int T0 = TH ? 1 : 0, T1 = TH ? 2 : 3, T2 = TH ? 5 : 4, T3 = TH ? 6 : 7, NF = T3 + 1;
#pragma unroll 1
  for (int jj = 0; jj < 4; ++jj) {
    v4u Fv[NF], Gv[NF];
#pragma unroll
    for (int i = 0; i < NF; ++i) {
      v2u a = *lp<v2u>(fa + 512 * (7 - i)), b = *lp<v2u>(fa + 512 * (7 - i) + 8);
      Fv[i] = v4u{a.x, a.y, b.x, b.y};
      Gv[i] = *lp<v4u>(ga + 512 * i);
    }
#pragma unroll
    for (int i = 0; i < NF; ++i) {
      if (i <= T0) acc[0] = mfma16h(Fv[i], Gv[T0 - i], acc[0]);
      if (i <= T1) acc[1] = mfma16h(Fv[i], Gv[T1 - i], acc[1]);
      if (i <= T2) acc[2] = mfma16h(Fv[i], Gv[T2 - i], acc[2]);
      if (i <= T3) acc[3] = mfma16h(Fv[i], Gv[T3 - i], acc[3]);
    }
    fa -= 64; ga -= 64;
  }
}

__global__ __launch_bounds__(512, 2) void s4d_mlp_gate_kernel(Params p) {
  __shared__ __attribute__((aligned(16))) char smem[163840];
  asm volatile("" :: "v"((LAS char*)smem) : "memory");
  const unsigned tid = threadIdx.x, lane = tid & 63;
  const unsigned w = __builtin_amdgcn_readfirstlane(tid >> 6);
  const unsigned xcd = blockIdx.x & 7, idx = blockIdx.x >> 3;
  STAMP(0);
  {
    u64 pc; asm volatile("s_getpc_b64 %0" : "=s"(pc));
    const rsrc_t rC = mkrs((const void*)(pc & ~255ull), CODE_PF_BYTES);
    asm volatile("s_mov_b32 m0, %0\n\ts_nop 4\n\tbuffer_load_dword %1, %2, 0 offen lds" :: "s"(98304 + w * 2048),
                 "v"(lane < 4 ? (((idx & 7) * 8 + w) * 4 + lane) * 128 : 0x7fffffffu), "s"(rC) : "memory");
    SB();
  }
  const rsrc_t rW = mkrs(p.ws, WS_END);
  const rsrc_t rX = mkrs(p.x, LT * DM * 2);
  const rsrc_t rW2 = mkrs(p.w2, FF * DM * 2);
#define PROLOG_LOADS() v2u Ev; ALD_B64S(Ev, 0u, rW, 0u)

  if (idx < 4) {
    PROLOG_LOADS();
    const unsigned hid = xcd * 4 + idx, tok0 = hid * 64 + w * 8;
    v4u xr[8];
    const unsigned vo = lane < 56 ? lane * 16 : 0x7fffffffu;
#pragma unroll
    for (int r = 0; r < 8; ++r) xr[r] = __builtin_amdgcn_raw_buffer_load_b128(rX, vo, (tok0 + r) * 896, 0);
    unsigned tch = __builtin_amdgcn_raw_buffer_load_b32(rW2, lane < 49 ? lane * 128 : 0x7fffffffu, (hid * 8 + w) * 49 * 128, 0);
    asm volatile("s_waitcnt vmcnt(9)" : "+v"(Ev) :: "memory");
    const u64 tag = (((u64)(unsigned)__builtin_amdgcn_readfirstlane(Ev.y) << 32) | (unsigned)__builtin_amdgcn_readfirstlane(Ev.x)) + 1;
    const unsigned tlo = (unsigned)tag, thi = (unsigned)(tag >> 32);
    float d[8][8], mean[8], mb[8], rs1[8];
#pragma unroll
    for (int r = 0; r < 8; ++r) {
      float s = 0.f;
#pragma unroll
      for (int i = 0; i < 4; ++i) s = dot2one(xr[r][i], s);
      mean[r] = wave_sum(s) * (1.0f / DM);
    }
    STAMP(1);
    float myboa = 0.f, myinva = 0.f;
#pragma unroll
    for (int r = 0; r < 8; ++r) {
      mb[r] = rbf(mean[r]);
      const float ml = lane < 56 ? mb[r] : 0.f;
      float q = 0.f;
#pragma unroll
      for (int i = 0; i < 4; ++i) {
        d[r][2 * i] = bflo(xr[r][i]) - ml; d[r][2 * i + 1] = bfhi(xr[r][i]) - ml;
        q = __builtin_fmaf(d[r][2 * i], d[r][2 * i], q); q = __builtin_fmaf(d[r][2 * i + 1], d[r][2 * i + 1], q);
      }
      const float dm = mean[r] - mb[r];
      const float var = fmaxf(wave_sum(q) * (1.0f / DM) - dm * dm, 0.f);
      const float sd1 = rbf(__builtin_amdgcn_sqrtf(rbf(rbf(var) + 1e-5f)));
      rs1[r] = __builtin_amdgcn_rcpf(sd1);
      const float mb2 = rbf(dm * rs1[r]);
      const float sd2 = rbf(__builtin_amdgcn_sqrtf(rbf(rbf(var * rs1[r] * rs1[r]) + 1e-5f)));
      if (lane == r) { myboa = -(mb[r] + mb2 * sd1); myinva = sd1 * sd2; }
    }
    st128s(v4u{asu(myboa), tlo, asu(myinva), tlo}, rW, lane < 8 ? lane * 16 : 0x70000000u, WS_ST + tok0 * 16);
    STAMP(2);
#pragma unroll
    for (int i = 0; i < 4; ++i) {
      unsigned o[8];
#pragma unroll
      for (int r = 0; r < 8; ++r) {
        unsigned t = pkbf(d[r][2 * i], d[r][2 * i + 1]);
        t = pkbf(bflo(t) * rs1[r], bfhi(t) * rs1[r]);
        o[r] = pkh(bflo(t), bfhi(t));
      }
      if (lane < 56) {
        const unsigned row = (lane * 4 + i) * 256;
        *lp<v4u>(row + (((2 * w) ^ (lane & 7)) << 4)) = v4u{o[0], o[1], o[2], o[3]};
        *lp<v4u>(row + (((2 * w + 1) ^ (lane & 7)) << 4)) = v4u{o[4], o[5], o[6], o[7]};
      }
    }
    BAR();
    STAMP(3);
#pragma unroll
    for (int k = 0; k < 7; ++k) {
      const unsigned sidx = k * 512 + tid, pair = sidx >> 4, pos = sidx & 15;
      v4u v = *lp<v4u>(sidx * 16);
      st128s(v, rW, pair * 8192 + ((pos ^ ((pair >> 2) & 7)) << 4), WS_XN + hid * 256);
    }
    asm volatile("s_waitcnt vmcnt(0)" : "+v"(tch) :: "memory");
    BAR();
    STAMP(4);
    if (tid == 0) st64s(v2u{tlo, thi}, rW, 0, WS_FX + hid * 8);
    if (hid == 0) {
      unsigned vo2 = tid < 224 ? WS_FH + ((tid / 14) * 4 * 256) + (tid % 14) * 16 : WS_FX + (tid & 31) * 8;
      spin64(v2u{tlo + 1, thi}, tid < 256, rW, vo2, tlo, thi);
      BAR();
      if (tid == 0) st64s(v2u{tlo, thi}, rW, 0, WS_E);
    }
    STAMP(5);
  } else {
    PROLOG_LOADS();
    const unsigned m = idx - 4, ma = m / 7, mb_ = m % 7;
    const unsigned tt = 4 * (xcd >> 1) + ma, sl = 7 * (xcd & 1) + mb_, tb = 4 * xcd + ma, cb = mb_, cp = 28 * xcd + m;
    const unsigned rot = (mb_ + 2 * ma) % 7;
    const unsigned tg = w & 3, hg = w >> 2, hf = lane >> 5, l31 = lane & 31, l15 = lane & 15, q4 = lane >> 4;
    unsigned plA, pAi, pCr, pCi, pb1a, pb1b;
    {
      const unsigned vo = tid * 2, so = cp * 1024;
      ALD_U16(plA, vo, mkrs(p.logA, DM * 512), so);
      ALD_U16(pAi, vo, mkrs(p.Aim, DM * 512), so);
      ALD_U16(pCr, vo, mkrs(p.Cr, DM * 512), so);
      ALD_U16(pCi, vo, mkrs(p.Ci, DM * 512), so);
      const rsrc_t rB1 = mkrs(p.b1, FF * 2);
      ALD_U16(pb1a, l31 * 2, rB1, sl * 256 + hg * 128);
      ALD_U16(pb1b, l31 * 2, rB1, sl * 256 + hg * 128 + 64);
    }
    const rsrc_t rW1 = mkrs(p.w1, DM * FF * 2);
    const unsigned vx0 = (lane >> 3) * 896 + (((lane & 7) ^ (lane >> 4)) << 4);
    const unsigned vx1 = (lane >> 3) * 896 + (((lane & 7) ^ (4 + (lane >> 4))) << 4) + 8 * 896;
    const unsigned vw1 = q4 * 3584 + ((l15 ^ (q4 << 2)) << 4);
    const unsigned vw2 = (lane >> 3) * 896 + (((lane & 7) ^ (((lane >> 4) & 1) << 2)) << 4);
    auto chunk_of = [&](int i) { unsigned c = i + rot; return c >= 7 ? c - 7 : c; };
    auto issueD = [&](int i) {
      const unsigned c = chunk_of(i), dst = (i & 3) * 32768 + w * 2048;
      const unsigned sx = (tt * 128 + w * 16) * 896 + c * 128, sw = (c * 64 + w * 8) * 3584 + sl * 256;
      dma16(rX, vx0, sx, dst); dma16(rX, vx1, sx, dst + 1024);
      dma16(rW1, vw1, sw, dst + 16384); dma16(rW1, vw1, sw + 4 * 3584, dst + 16384 + 1024);
    };
    auto wslot = [&](int i) { return (i % 3 == 0 ? 3u : (unsigned)(i % 3 - 1)) * 32768 + w * 4096; };
    auto issueW = [&](int i) {
      const unsigned s = ((w * 7 + chunk_of(i)) * 32) * 896 + cb * 128, dst = wslot(i);
#pragma unroll
      for (int j = 0; j < 4; ++j) dma16(rW2, vw2, s + j * 8 * 896, dst + j * 1024);
    };
    issueD(0); issueD(1);
    const unsigned ldtp = ((const CAS unsigned*)p.ldt)[cp], dskp = ((const CAS unsigned*)p.Dsk)[cp];
    const float dt0 = rbf(expf(bflo(ldtp))), dt1 = rbf(expf(bfhi(ldtp)));
    asm volatile("s_waitcnt vmcnt(8)" : "+v"(Ev), "+v"(plA), "+v"(pAi), "+v"(pCr), "+v"(pCi), "+v"(pb1a), "+v"(pb1b) :: "memory");
    STAMP(1);
    const u64 tag = (((u64)(unsigned)__builtin_amdgcn_readfirstlane(Ev.y) << 32) | (unsigned)__builtin_amdgcn_readfirstlane(Ev.x)) + 1;
    const unsigned tlo = (unsigned)tag, thi = (unsigned)(tag >> 32);

    float ysc0, ysc1;
    {
      const float dt = hg ? dt1 : dt0;
      const float l2 = -rbf(__builtin_amdgcn_exp2f(bflo(plA) * 1.4426950408889634f)) * dt * 1.4426950408889634f;
      const float th = bflo(pAi) * dt * 0.15915494309189535f;
      auto cexp = [&](float s) {
        const float mg = __builtin_amdgcn_exp2f(l2 * s), f = __builtin_amdgcn_fractf(th * s);
        return c32{mg * __builtin_amdgcn_cosf(f), mg * __builtin_amdgcn_sinf(f)};
      };
      unsigned ew = wave_maxu(max(pCr & 0x7fffu, pCi & 0x7fffu)) >> 7;
      ew = min(max(ew, 1u), 253u);
      const float inv = asf((254u - ew) << 23);
      const c32 w1c = cexp(1.f), w128 = cexp(128.f);
      c32 w8 = cexp(8.f); w8.i = -w8.i;
      const c32 w2c = cmul(w1c, w1c), w4c = cmul(w2c, w2c);
      const unsigned sb = SPARE + w * 4096;
      auto put = [&](int row, c32 v) { *lp<unsigned>(sb + row * 256 + ((lane * 4) ^ ((row & 7) << 5))) = pkh(v.r, v.i); };
      auto frag = [&](int s) { return *lp<v4u>(sb + l15 * 256 + (((4 * s + q4) ^ (2 * (l15 & 7))) << 4)); };
      v4u Bf[4];
      {
        const c32 w16 = cmul(w8, w8);
        c32 e = {1.f, 0.f}, o = w8;
#pragma unroll
        for (int j = 0; j < 8; ++j) {
          put(2 * j, e); put(2 * j + 1, o);
          if (j == 0) e = w16; else if (j < 7) e = cmul(e, w16);
          if (j < 7) o = cmul(o, w16);
        }
#pragma unroll
        for (int s = 0; s < 4; ++s) Bf[s] = frag(s);
      }
      f4 kc[3];
      c32 b0 = {bflo(pCr) * inv, bflo(pCi) * inv}, b1c = cmul(b0, w128), bs = cmul(b1c, w128);
      {
        c32 u = b0, v = b1c;
#pragma unroll
        for (int j = 0; j < 8; ++j) {
          put(j, u); put(8 + j, v);
          if (j < 7) { u = cmul(u, w1c); v = cmul(v, w1c); }
        }
        kc[0] = f4{0.f, 0.f, 0.f, 0.f};
#pragma unroll
        for (int s = 0; s < 4; ++s) kc[0] = mfma16h(frag(s), Bf[s], kc[0]);
      }
      SB(); issueD(2); SB();
      {
#pragma unroll
        for (int i = 0; i < 2; ++i) {
          c32 u = bs; bs = cmul(bs, w128);
#pragma unroll
          for (int j = 0; j < 4; ++j) { put(4 * i + j, u); if (j < 3) u = cmul(u, w2c); }
        }
#pragma unroll
        for (int i = 0; i < 4; ++i) { put(8 + 2 * i, bs); put(9 + 2 * i, cmul(bs, w4c)); bs = cmul(bs, w128); }
        kc[1] = f4{0.f, 0.f, 0.f, 0.f};
#pragma unroll
        for (int s = 0; s < 4; ++s) kc[1] = mfma16h(frag(s), Bf[s], kc[1]);
#pragma unroll
        for (int i = 0; i < 9; ++i) { put(i, bs); if (i < 8) bs = cmul(bs, w128); }
        kc[2] = f4{0.f, 0.f, 0.f, 0.f};
#pragma unroll
        for (int s = 0; s < 4; ++s) kc[2] = mfma16h(frag(s), Bf[s], kc[2]);
      }
      const float scw = asf(ew << 23);
#pragma unroll
      for (int t = 0; t < 3; ++t) {
        f4 v = kc[t] * scw;
        if (t == 2 && lane == 63) v.w = scw;
        *lp<f4>(sb + t * 1024 + lane * 16) = v;
      }
      STAMP(2);
      BAR();
      {
        unsigned e0 = 0, e1 = 0;
#pragma unroll
        for (int k = 0; k < 4; ++k) { e0 = max(e0, *lp<unsigned>(SPARE + k * 4096 + 3068)); e1 = max(e1, *lp<unsigned>(SPARE + (4 + k) * 4096 + 3068)); }
        e0 = __builtin_amdgcn_readfirstlane(e0); e1 = __builtin_amdgcn_readfirstlane(e1);
        ysc0 = 2.f * dt0 * asf(e0); ysc1 = 2.f * dt1 * asf(e1);
        if (tg < 3) {
          const float invc = asf((254u << 23) - (hg ? e1 : e0));
          const unsigned pa = SPARE + hg * 16384 + tg * 1024 + lane * 16;
          f4 v = (*lp<f4>(pa) + *lp<f4>(pa + 4096)) + (*lp<f4>(pa + 8192) + *lp<f4>(pa + 12288));
          v *= invc;
          const bool fine = tg == 0 || (tg == 1 && q4 < 2);
          const unsigned sh = tg == 0 ? 3u : tg == 2 ? 0u : (q4 < 2 ? 2u : 1u);
          const unsigned ia = (tg == 0 ? 128 * (q4 >> 1) + 4 * (q4 & 1) : 256 * tg + 64 * q4) + (l15 << sh) + 768 * hg;
          const unsigned s1 = fine ? 2u : 32u, s0 = tg == 2 ? 16u : 1u;
#pragma unroll
          for (int i = 0; i < 4; ++i) {
            const unsigned ix = ia + (i >> 1) * s1 + (i & 1) * s0;
            *lp<_Float16>(SPARE + 3072 + 2 * ix + 3072 * (ix >> 9)) = (_Float16)v[i];
          }
        }
      }
      BAR();
      {
        const unsigned t = tid & 255;
        unsigned v[9];
#pragma unroll
        for (int e = 0; e < 9; ++e) {
          const int lag = 2047 - 8 * (int)t - e;
          unsigned ix = lag < 256 ? (unsigned)max(lag, 0) : (pkbf((float)lag, 0.f) & 0xffffu) - 0x4300u + 128u;
          ix += 768 * hg;
          v[e] = *lp<u16>(SPARE + 3072 + 2 * ix + 3072 * (ix >> 9));
        }
        if (t == 255) v[8] = 0;
        const unsigned kb = KREV + hg * 2 * KCP + t * 16;
        *lp<v4u>(kb) = v4u{v[1] | v[2] << 16, v[3] | v[4] << 16, v[5] | v[6] << 16, v[7] | v[8] << 16};
        *lp<v4u>(kb + KCP) = v4u{v[0] | v[1] << 16, v[2] | v[3] << 16, v[4] | v[5] << 16, v[6] | v[7] << 16};
        if (tid < 16) *lp<v4u>(KREV + (tid >> 2) * KCP + 4096 + (tid & 3) * 16) = v4u{0, 0, 0, 0};
      }
    }
    SB(); issueD(3); SB();
    STAMP(3);

    f16v acc[2];
#pragma unroll
    for (int i = 0; i < 16; ++i) { acc[0][i] = 0.f; acc[1][i] = 0.f; }
    float cs0 = 0.f, cs1 = 0.f;
    v4u stg;
    {
      unsigned aB[4], aA[2][4];
      const unsigned rowB = tg * 32 + l31, qa = l15 >> 2, pp = lane & 3, blk = (lane >> 4) & 1;
#pragma unroll
      for (int i = 0; i < 4; ++i) {
        const unsigned ks = (i + tg) & 3;
        aB[i] = rowB * 128 + (((2 * ks + hf) ^ ((rowB >> 1) & 7)) << 4);
#pragma unroll
        for (int mm = 0; mm < 2; ++mm)
          aA[mm][i] = 16384 + (16 * ks + 8 * hf + qa) * 256 + (((4 * (2 * hg + mm) + 2 * blk + (pp >> 1)) ^ (qa << 2)) << 4) + 8 * (pp & 1);
      }
#pragma unroll
      for (int i = 0; i < 7; ++i) {
        if (i == 0) VMW(12); else if (i <= 4) VMW(8); else VMW(9);
        BAR();
        if (i == 0) STAMP(4);
        if (i >= 1 && i <= 3) issueD(i + 3);
        if (i == 4) ALD_B128S(stg, l31 * 16, rW, WS_ST + (tt * 128 + tg * 32) * 16);
        if (i >= 4) issueW(i - 4);
        const unsigned so = (i & 3) * 32768;
#pragma unroll
        for (int k = 0; k < 4; ++k) {
          const v4u B = *lp<v4u>(so + aB[k]);
          const v4u A0 = trfrag(so + aA[0][k], so + aA[0][k] + 1024), A1 = trfrag(so + aA[1][k], so + aA[1][k] + 1024);
          acc[0] = mfma32(A0, B, acc[0]); acc[1] = mfma32(A1, B, acc[1]);
          if (k == 0) {
#pragma unroll
            for (int j = 0; j < 4; ++j) { cs0 += bflo(A0[j]); cs0 += bfhi(A0[j]); cs1 += bflo(A1[j]); cs1 += bfhi(A1[j]); }
          }
        }
      }
    }
    *lp<f2>(SPARE + w * 512 + lane * 8) = f2{cs0, cs1};
    BAR();
    STAMP(5);
    float av;
    {
      const unsigned ca = SPARE + hg * 2048 + lane * 8;
      f2 c = (*lp<f2>(ca) + *lp<f2>(ca + 512)) + (*lp<f2>(ca + 1024) + *lp<f2>(ca + 1536));
      if (tid < 112) *lp<v4u>(UARR + (tid / 28) * UCP + (tid % 28) * 16) = v4u{0, 0, 0, 0};
      else if (tid < 114) *lp<unsigned>(UARR + (tid - 112) * 2 * UCP + 448) = 0;
      auto s0 = __builtin_amdgcn_permlane32_swap(asu(c.x), asu(c.x), false, false);
      auto s1 = __builtin_amdgcn_permlane32_swap(asu(c.y), asu(c.y), false, false);
      float t0 = asf(s0[0]) + asf(s0[1]), t1 = asf(s1[0]) + asf(s1[1]);
      asm volatile("" : "+v"(t0), "+v"(t1));
      const float A0 = hf ? bflo(pb1a) : t0, A1 = hf ? bflo(pb1b) : t1;
      asm volatile("s_waitcnt vmcnt(12)" : "+v"(stg) :: "memory");
#pragma unroll 1
      for (unsigned n = 0; n < SPIN_CAP; ++n) {
        if (__all(stg.y == tlo && stg.w == tlo)) break;
        __builtin_amdgcn_s_sleep(2);
        asm volatile("buffer_load_dwordx4 %0, %1, %2, %3 offen sc1\n\ts_waitcnt vmcnt(0)" : "=&v"(stg) : "v"(l31 * 16), "s"(rW), "s"(WS_ST + (tt * 128 + tg * 32) * 16) : "memory");
      }
      const float Bv = asf(hf ? stg.z : stg.x);
      av = __builtin_amdgcn_rcpf(asf(stg.z));
      acc[0] = __builtin_amdgcn_mfma_f32_32x32x2f32(A0, Bv, acc[0], 0, 0, 0);
      acc[1] = __builtin_amdgcn_mfma_f32_32x32x2f32(A1, Bv, acc[1], 0, 0, 0);
    }
    STAMP(6);
    v4u W[7][4];
    const unsigned aW0 = (4 * hf + (l15 >> 2)) * 128 + (((2 * ((lane >> 4) & 1) + ((lane & 3) >> 1)) ^ (((l15 >> 3) & 1) << 2)) << 4) + 8 * (lane & 1);
    auto readW = [&](int i) {
      const unsigned b = wslot(i);
#pragma unroll
      for (int s = 0; s < 2; ++s)
#pragma unroll
        for (int mc = 0; mc < 2; ++mc) { const unsigned a = b + s * 2048 + (aW0 ^ (mc * 64)); W[i][2 * s + mc] = trfrag(a, a + 1024); }
    };
    auto gelu_q = [&](int qq) {
      unsigned o[4];
#pragma unroll
      for (int j = 0; j < 4; ++j) {
        float r[2];
#pragma unroll
        for (int e = 0; e < 2; ++e) {
          const float v = acc[qq >> 1][8 * (qq & 1) + 2 * j + e] * av;
          const float z = v * __builtin_fmaf(v * v, -0.10294324f, -2.3022082f);
          r[e] = v * __builtin_amdgcn_rcpf(1.0f + __builtin_amdgcn_exp2f(z));
        }
        o[j] = pkbf(r[0], r[1]);
      }
      st128s(v4u{o[0], o[1], o[2], o[3]}, rW, lane * 16, WS_H + ((4 * tt + tg) * 112 + 8 * sl + 4 * hg + qq) * 1024);
    };
    v2u xf;
    v4u uv;
    gelu_q(0); VMW(9); readW(0); LGK0(); SB(); issueW(3); SB();
    gelu_q(1); ALD_B64S(xf, l31 * 8, rW, WS_FX); VMW(11); readW(1); LGK0(); SB(); issueW(4); SB();
    gelu_q(2); SB();
    gelu_q(3);
    STAMP(7);
    asm volatile("s_waitcnt vmcnt(6)" : "+v"(xf) :: "memory");
    spin64(xf, true, rW, WS_FX + l31 * 8, tlo, thi);
    ALD_B128S(uv, tid * 16, rW, WS_XN + cp * 8192);
    readW(2); readW(3); LGK0(); SB(); issueW(5); issueW(6); SB();
    STAMP(8);
    asm volatile("s_waitcnt vmcnt(8)" : "+v"(uv) :: "memory");
    if (lane == 0) st64s(v2u{tlo, thi}, rW, 0, WS_FH + (4 * tt + tg) * 256 + (2 * sl + hg) * 8);
    readW(4);
    {
      const unsigned a0 = __builtin_amdgcn_perm(uv.y, uv.x, 0x05040100), a1 = __builtin_amdgcn_perm(uv.w, uv.z, 0x05040100);
      const unsigned c0 = __builtin_amdgcn_perm(uv.y, uv.x, 0x07060302), c1 = __builtin_amdgcn_perm(uv.w, uv.z, 0x07060302);
      const unsigned ub = UARR + 448 + tid * 8;
      *lp<unsigned>(ub + 4) = a0; *lp<unsigned>(ub + 8) = a1;
      *lp<v2u>(ub + UCP) = v2u{a0, a1};
      *lp<unsigned>(ub + 2 * UCP + 4) = c0; *lp<unsigned>(ub + 2 * UCP + 8) = c1;
      *lp<v2u>(ub + 3 * UCP) = v2u{c0, c1};
    }
    BAR();
    STAMP(9);
    v2u hfl;
    {
      const unsigned jh = w & 1, thh = (w >> 1) & 1;
      f4 ya[4];
#pragma unroll
      for (int i = 0; i < 4; ++i) ya[i] = f4{0.f, 0.f, 0.f, 0.f};
      const unsigned fa = KREV + (hg * 2 + (l15 & 1)) * KCP + 2 * (2044 - 1792 - 128 * jh - 4 * (l15 >> 1) + 8 * q4);
      const unsigned ga = UARR + (hg * 2 + (l15 & 1)) * UCP + 2 * (224 - 128 * jh + 32 * (l15 >> 1) + 8 * q4);
      if (thh) conv_loop<1>(fa, ga, ya); else conv_loop<0>(fa, ga, ya);
      STAMP(10);
      ALD_B64S(hfl, lane * 8, rW, WS_FH + tb * 512);
      VMW(2); readW(5); readW(6);
      BAR();
      const unsigned yb = (hg * 2 + jh) * YCH + 4 * (36 * (l15 >> 1) + 2 * (l15 & 1) + 8 * q4);
#pragma unroll
      for (int i = 0; i < 4; ++i) {
        const unsigned t = thh ? (i == 0 ? 1 : i == 1 ? 2 : i == 2 ? 5 : 6) : (i == 0 ? 0 : i == 1 ? 3 : i == 2 ? 4 : 7);
        *lp<f2>(yb + t * 1152) = f2{ya[i].x, ya[i].y};
        *lp<f2>(yb + t * 1152 + 16) = f2{ya[i].z, ya[i].w};
      }
      BAR();
      const unsigned yr = tid * 16 + (tid >> 3) * 16;
      const f4 y0 = (*lp<f4>(yr) + *lp<f4>(yr + YCH)) * ysc0, y1 = (*lp<f4>(yr + 2 * YCH) + *lp<f4>(yr + 3 * YCH)) * ysc1;
      BAR();
      const float D0 = bflo(dskp), D1 = bfhi(dskp);
      v4u sv;
#pragma unroll
      for (int i = 0; i < 4; ++i) {
        const unsigned yy = pkbf(y0[i], y1[i]), ud = pkbf(hlo(uv[i]) * D0, hhi(uv[i]) * D1);
        sv[i] = pkbf(bflo(yy) + bflo(ud), bfhi(yy) + bfhi(ud));
      }
      st128s(sv, rW, tid * 16, WS_S + cp * 8192);
    }
    STAMP(11);
    {
      asm volatile("s_waitcnt vmcnt(1)" : "+v"(hfl) :: "memory");
      const bool need = l31 >= ((14 * w) >> 2) && l31 <= ((14 * w + 13) >> 2);
      spin64(hfl, need, rW, WS_FH + tb * 512 + lane * 8, tlo, thi);
    }
    STAMP(12);
    const unsigned etok = tb * 64 + (tid >> 3), ech = cb * 64 + (tid & 7) * 8;
    v4u H[14];
    auto loadH = [&](int half, int k) {
      return __builtin_amdgcn_raw_buffer_load_b128(rW, lane * 16, WS_H + ((2 * tb + half) * 112 + 14 * w + 2 * chunk_of(k >> 1) + (k & 1)) * 1024, 16);
    };
#pragma unroll
    for (int k = 0; k < 14; ++k) H[k] = loadH(0, k);
    SB();
    VMW(14);
    if (lane == 0) st64s(v2u{tlo, thi}, rW, 0, WS_FS + ((cp >> 5) * 8 + w) * 256 + (cp & 31) * 8);
    SB();
    const unsigned pw = w * 64 * PROW + l31 * PROW + hf * 16;
    v2u sfl;
    v4u xe, b2e;
    unsigned se[4];
#pragma unroll
    for (int half = 0; half < 2; ++half) {
      f16v o0, o1;
#pragma unroll
      for (int i = 0; i < 16; ++i) { o0[i] = 0.f; o1[i] = 0.f; }
#pragma unroll
      for (int k = 0; k < 14; ++k) {
        o0 = mfma32(W[k >> 1][2 * (k & 1)], H[k], o0);
        o1 = mfma32(W[k >> 1][2 * (k & 1) + 1], H[k], o1);
        if (half == 0) H[k] = loadH(1, k);
        SB();
      }
      if (half == 0) {
        xe = __builtin_amdgcn_raw_buffer_load_b128(rX, etok * 896 + ech * 2, 0, 0);
        b2e = __builtin_amdgcn_raw_buffer_load_b128(mkrs(p.b2, DM * 2), ech * 2, 0, 0);
        sfl = __builtin_bit_cast(v2u, __builtin_amdgcn_raw_buffer_load_b64(rW, l31 * 8, WS_FS + (cb * 8 + (tb >> 2)) * 256, 16));
        STAMP(13);
      } else {
        STAMP(14);
        spin64(sfl, true, rW, WS_FS + (cb * 8 + (tb >> 2)) * 256 + l31 * 8, tlo, thi);
#pragma unroll
        for (int i = 0; i < 4; ++i) se[i] = __builtin_amdgcn_raw_buffer_load_b32(rW, ((ech >> 1) + i) * 8192 + etok * 4, WS_S, 16);
      }
#pragma unroll
      for (int g = 0; g < 4; ++g) {
        *lp<f4>(pw + half * 32 * PROW + g * 32) = f4{o0[4 * g], o0[4 * g + 1], o0[4 * g + 2], o0[4 * g + 3]};
        *lp<f4>(pw + half * 32 * PROW + g * 32 + 128) = f4{o1[4 * g], o1[4 * g + 1], o1[4 * g + 2], o1[4 * g + 3]};
      }
      SB();
    }
    BAR();
    STAMP(15);
    {
      const unsigned pr = (tid >> 3) * PROW + (tid & 7) * 32;
      f4 a = *lp<f4>(pr), b = *lp<f4>(pr + 16);
#pragma unroll
      for (int k = 1; k < 8; ++k) { a += *lp<f4>(pr + k * 64 * PROW); b += *lp<f4>(pr + k * 64 * PROW + 16); }
      v4u ov;
#pragma unroll
      for (int i = 0; i < 4; ++i) {
        const float p0 = i < 2 ? a[2 * i] : b[2 * i - 4], p1 = i < 2 ? a[2 * i + 1] : b[2 * i - 3];
        unsigned t = pkbf(p0, p1);
        t = pkbf(bflo(t) + bflo(b2e[i]), bfhi(t) + bfhi(b2e[i]));
        const float o0 = bflo(t), o1 = bfhi(t);
        t = pkbf(o0 * __builtin_amdgcn_rcpf(1.0f + __builtin_amdgcn_exp2f(o0 * -1.4426950408889634f)),
                 o1 * __builtin_amdgcn_rcpf(1.0f + __builtin_amdgcn_exp2f(o1 * -1.4426950408889634f)));
        t = pkbf(bflo(t) * bflo(se[i]), bfhi(t) * bfhi(se[i]));
        ov[i] = pkbf(bflo(xe[i]) + bflo(t), bfhi(xe[i]) + bfhi(t));
      }
      __builtin_amdgcn_raw_buffer_store_b128(ov, mkrs(p.out, LT * DM * 2), etok * 896 + ech * 2, 0, 0);
    }
    STAMP(16);
  }
}

extern "C" void kernel_launch(void* const* d_in, const int* in_sizes, int n_in, void* d_out, int out_size, void* d_ws, size_t ws_size,
                              hipStream_t stream) {
  Params p;
  p.x = (const u16*)d_in[0]; p.logA = (const u16*)d_in[1]; p.Aim = (const u16*)d_in[2]; p.Cr = (const u16*)d_in[3];
  p.Ci = (const u16*)d_in[4]; p.ldt = (const u16*)d_in[5]; p.Dsk = (const u16*)d_in[6]; p.w1 = (const u16*)d_in[8];
  p.b1 = (const u16*)d_in[9]; p.w2 = (const u16*)d_in[10]; p.b2 = (const u16*)d_in[11];
  p.out = (u16*)d_out; p.ws = (char*)d_ws;
  s4d_mlp_gate_kernel<<<256, 512, 0, stream>>>(p);
}
```

```cpp
#define ENC 99
#define ENCB 100
#define ENCC 0
#include <hip/hip_runtime.h>
#include <hip/hip_bf16.h>

typedef unsigned short u16;
typedef unsigned long long u64;
typedef unsigned v4u __attribute__((ext_vector_type(4)));
typedef unsigned v2u __attribute__((ext_vector_type(2)));
typedef float f2 __attribute__((ext_vector_type(2)));
typedef float f4 __attribute__((ext_vector_type(4)));
typedef float f16v __attribute__((ext_vector_type(16)));
typedef short s4 __attribute__((ext_vector_type(4)));
typedef __bf16 b8 __attribute__((ext_vector_type(8)));
typedef __bf16 b2 __attribute__((ext_vector_type(2)));
typedef _Float16 h8 __attribute__((ext_vector_type(8)));
typedef _Float16 h2 __attribute__((ext_vector_type(2)));
typedef __amdgpu_buffer_rsrc_t rsrc_t;
#define LAS __attribute__((address_space(3)))
#define CAS __attribute__((address_space(4)))
#define DEV __device__ __forceinline__
#define SB() __builtin_amdgcn_sched_barrier(0)
#define VMW(n) asm volatile("s_waitcnt vmcnt(" #n ")" ::: "memory")
#define LGK0() asm volatile("s_waitcnt lgkmcnt(0)" ::: "memory")
#define BAR() do { asm volatile("s_waitcnt lgkmcnt(0)\n\ts_barrier" ::: "memory"); } while (0)

#define STAMPS 0
#define STAMP(i) do { if (i == ENC) { SB(); asm volatile("s_memrealtime %0\n\ts_memtime %1\n\ts_waitcnt lgkmcnt(0)" : "=s"(ert), "=s"(ect) :: "memory"); SB(); } } while (0)

constexpr int LT = 2048, DM = 448, FF = 1792;
constexpr unsigned WS_E = 0, WS_FX = 1024, WS_FH = 4096, WS_FS = 32768, WS_ST = 65536, WS_XN = 131072,
                   WS_S = WS_XN + 224 * 8192, WS_H = WS_S + 224 * 8192, WS_END = WS_H + 64 * 112 * 1024, WS_DBG = 16u << 20;
constexpr unsigned SPARE = 131072, KREV = SPARE + 12288, KCP = 4224, UARR = 65536, UCP = 4640, YCH = 9216, PROW = 272;
constexpr unsigned CODE_PF_BYTES = 27648;
constexpr unsigned SPIN_CAP = 1u << 20;

struct Params {
  const u16 *x, *logA, *Aim, *Cr, *Ci, *ldt, *Dsk, *w1, *b1, *w2, *b2;
  u16* out;
  char* ws;
};

DEV rsrc_t mkrs(const void* p, unsigned bytes) { return __builtin_amdgcn_make_buffer_rsrc((void*)p, (short)0, (int)bytes, 0x00020000); }
DEV float asf(unsigned u) { return __builtin_bit_cast(float, u); }
DEV unsigned asu(float f) { return __builtin_bit_cast(unsigned, f); }
DEV float bflo(unsigned u) { return asf(u << 16); }
DEV float bfhi(unsigned u) { return asf(u & 0xffff0000u); }
DEV unsigned pkbf(float a, float b) { f2 v = {a, b}; return __builtin_bit_cast(unsigned, __builtin_convertvector(v, b2)); }
DEV unsigned pkh(float a, float b) { f2 v = {a, b}; return __builtin_bit_cast(unsigned, __builtin_convertvector(v, h2)); }
DEV float rbf(float a) { return bflo(pkbf(a, a)); }
DEV float hlo(unsigned u) { return (float)__builtin_bit_cast(h2, u).x; }
DEV float hhi(unsigned u) { return (float)__builtin_bit_cast(h2, u).y; }
template <typename T> DEV LAS T* lp(unsigned a) { return (LAS T*)(size_t)a; }
DEV void dma16(rsrc_t rs, unsigned voff, unsigned soff, unsigned lds) {
  asm volatile("s_mov_b32 m0, %0\n\ts_nop 0\n\tbuffer_load_dwordx4 %1, %2, %3 offen lds" :: "s"(lds), "v"(voff), "s"(rs), "s"(soff) : "memory");
}
#define ALD_U16(d, vo, rs, so) asm volatile("buffer_load_ushort %0, %1, %2, %3 offen" : "=v"(d) : "v"(vo), "s"(rs), "s"(so) : "memory")
#define ALD_B32(d, vo, rs, so) asm volatile("buffer_load_dword %0, %1, %2, %3 offen" : "=v"(d) : "v"(vo), "s"(rs), "s"(so) : "memory")
#define ALD_B64S(d, vo, rs, so) asm volatile("buffer_load_dwordx2 %0, %1, %2, %3 offen sc1" : "=v"(d) : "v"(vo), "s"(rs), "s"(so) : "memory")
#define ALD_B128S(d, vo, rs, so) asm volatile("buffer_load_dwordx4 %0, %1, %2, %3 offen sc1" : "=v"(d) : "v"(vo), "s"(rs), "s"(so) : "memory")
DEV void st128s(v4u v, rsrc_t rs, unsigned vo, unsigned so) { __builtin_amdgcn_raw_buffer_store_b128(v, rs, vo + so, 0, 16); }
DEV float dot2one(unsigned x, float s) { const b2 ones = {(__bf16)1.0f, (__bf16)1.0f}; return __builtin_amdgcn_fdot2_f32_bf16(__builtin_bit_cast(b2, x), ones, s, false); }
DEV void st64s(v2u v, rsrc_t rs, unsigned vo, unsigned so) { __builtin_amdgcn_raw_buffer_store_b64(v, rs, vo, so, 16); }
DEV v4u trfrag(unsigned a0, unsigned a1) {
  s4 lo = __builtin_amdgcn_ds_read_tr16_b64_v4i16(lp<s4>(a0)), hi = __builtin_amdgcn_ds_read_tr16_b64_v4i16(lp<s4>(a1));
  v2u l = __builtin_bit_cast(v2u, lo), h = __builtin_bit_cast(v2u, hi);
  return v4u{l.x, l.y, h.x, h.y};
}
DEV f16v mfma32(v4u a, v4u b, f16v c) { return __builtin_amdgcn_mfma_f32_32x32x16_bf16(__builtin_bit_cast(b8, a), __builtin_bit_cast(b8, b), c, 0, 0, 0); }
DEV f4 mfma16h(v4u a, v4u b, f4 c) { return __builtin_amdgcn_mfma_f32_16x16x32_f16(__builtin_bit_cast(h8, a), __builtin_bit_cast(h8, b), c, 0, 0, 0); }
template <int C, int RM> DEV float dppf(float v) { return asf(__builtin_amdgcn_update_dpp(0, asu(v), C, RM, 0xf, true)); }
DEV float wave_sum(float v) {
  v += dppf<0xB1, 0xf>(v); v += dppf<0x4E, 0xf>(v); v += dppf<0x141, 0xf>(v); v += dppf<0x140, 0xf>(v);
  v += dppf<0x142, 0xa>(v); v += dppf<0x143, 0xc>(v);
  return asf(__builtin_amdgcn_readlane(asu(v), 63));
}
template <int C, int RM> DEV unsigned dppu(unsigned v) { return __builtin_amdgcn_update_dpp(0, v, C, RM, 0xf, true); }
DEV unsigned wave_maxu(unsigned v) {
  v = max(v, dppu<0xB1, 0xf>(v)); v = max(v, dppu<0x4E, 0xf>(v)); v = max(v, dppu<0x141, 0xf>(v)); v = max(v, dppu<0x140, 0xf>(v));
  v = max(v, dppu<0x142, 0xa>(v)); v = max(v, dppu<0x143, 0xc>(v));
  return __builtin_amdgcn_readlane(v, 63);
}
DEV void spin64(v2u f, bool need, rsrc_t rs, unsigned vo, unsigned tlo, unsigned thi) {
#pragma unroll 1
  for (unsigned n = 0; n < SPIN_CAP; ++n) {
    if (__all(!need || (f.x == tlo && f.y == thi))) break;
    __builtin_amdgcn_s_sleep(2);
    asm volatile("buffer_load_dwordx2 %0, %1, %2, 0 offen sc1\n\ts_waitcnt vmcnt(0)" : "=&v"(f) : "v"(vo), "s"(rs) : "memory");
  }
}
struct c32 { float r, i; };
DEV c32 cmul(c32 a, c32 b) { return c32{__builtin_fmaf(a.r, b.r, -a.i * b.i), __builtin_fmaf(a.r, b.i, a.i * b.r)}; }

template <int TH> DEV void conv_loop(unsigned fa, unsigned ga, f4 (&acc)[4]) {
  constexpr int T0 = TH ? 1 : 0, T1 = TH ? 2 : 3, T2 = TH ? 5 : 4, T3 = TH ? 6 : 7, NF = T3 + 1;
#pragma unroll 1
  for (int jj = 0; jj < 4; ++jj) {
    v4u Fv[NF], Gv[NF];
#pragma unroll
    for (int i = 0; i < NF; ++i) {
      v2u a = *lp<v2u>(fa + 512 * (7 - i)), b = *lp<v2u>(fa + 512 * (7 - i) + 8);
      Fv[i] = v4u{a.x, a.y, b.x, b.y};
      Gv[i] = *lp<v4u>(ga + 512 * i);
    }
#pragma unroll
    for (int i = 0; i < NF; ++i) {
      if (i <= T0) acc[0] = mfma16h(Fv[i], Gv[T0 - i], acc[0]);
      if (i <= T1) acc[1] = mfma16h(Fv[i], Gv[T1 - i], acc[1]);
      if (i <= T2) acc[2] = mfma16h(Fv[i], Gv[T2 - i], acc[2]);
      if (i <= T3) acc[3] = mfma16h(Fv[i], Gv[T3 - i], acc[3]);
    }
    fa -= 64; ga -= 64;
  }
}

__global__ __launch_bounds__(512, 2) void s4d_mlp_gate_kernel(Params p) {
  __shared__ __attribute__((aligned(16))) char smem[163840];
  asm volatile("" :: "v"((LAS char*)smem) : "memory");
  const unsigned tid = threadIdx.x, lane = tid & 63;
  const unsigned w = __builtin_amdgcn_readfirstlane(tid >> 6);
  const unsigned xcd = blockIdx.x & 7, idx = blockIdx.x >> 3;
  u64 ert0, ect0, ert = 0, ect = 0;
  asm volatile("s_memrealtime %0\n\ts_memtime %1\n\ts_waitcnt lgkmcnt(0)" : "=s"(ert0), "=s"(ect0) :: "memory");
  STAMP(0);
  {
    u64 pc; asm volatile("s_getpc_b64 %0" : "=s"(pc));
    const rsrc_t rC = mkrs((const void*)(pc & ~255ull), CODE_PF_BYTES);
    asm volatile("s_mov_b32 m0, %0\n\ts_nop 4\n\tbuffer_load_dword %1, %2, 0 offen lds" :: "s"(98304 + w * 2048),
                 "v"(lane < 4 ? (((idx & 7) * 8 + w) * 4 + lane) * 128 : 0x7fffffffu), "s"(rC) : "memory");
    SB();
  }
  const rsrc_t rW = mkrs(p.ws, WS_END);
  const rsrc_t rX = mkrs(p.x, LT * DM * 2);
  const rsrc_t rW2 = mkrs(p.w2, FF * DM * 2);
#define PROLOG_LOADS() v2u Ev; ALD_B64S(Ev, 0u, rW, 0u)

  if (idx < 4) {
    PROLOG_LOADS();
    const unsigned hid = xcd * 4 + idx, tok0 = hid * 64 + w * 8;
    v4u xr[8];
    const unsigned vo = lane < 56 ? lane * 16 : 0x7fffffffu;
#pragma unroll
    for (int r = 0; r < 8; ++r) xr[r] = __builtin_amdgcn_raw_buffer_load_b128(rX, vo, (tok0 + r) * 896, 0);
    unsigned tch = __builtin_amdgcn_raw_buffer_load_b32(rW2, lane < 49 ? lane * 128 : 0x7fffffffu, (hid * 8 + w) * 49 * 128, 0);
    asm volatile("s_waitcnt vmcnt(9)" : "+v"(Ev) :: "memory");
    const u64 tag = (((u64)(unsigned)__builtin_amdgcn_readfirstlane(Ev.y) << 32) | (unsigned)__builtin_amdgcn_readfirstlane(Ev.x)) + 1;
    const unsigned tlo = (unsigned)tag, thi = (unsigned)(tag >> 32);
    float d[8][8], mean[8], mb[8], rs1[8];
#pragma unroll
    for (int r = 0; r < 8; ++r) {
      float s = 0.f;
#pragma unroll
      for (int i = 0; i < 4; ++i) s = dot2one(xr[r][i], s);
      mean[r] = wave_sum(s) * (1.0f / DM);
    }
    STAMP(1);
    float myboa = 0.f, myinva = 0.f;
#pragma unroll
    for (int r = 0; r < 8; ++r) {
      mb[r] = rbf(mean[r]);
      const float ml = lane < 56 ? mb[r] : 0.f;
      float q = 0.f;
#pragma unroll
      for (int i = 0; i < 4; ++i) {
        d[r][2 * i] = bflo(xr[r][i]) - ml; d[r][2 * i + 1] = bfhi(xr[r][i]) - ml;
        q = __builtin_fmaf(d[r][2 * i], d[r][2 * i], q); q = __builtin_fmaf(d[r][2 * i + 1], d[r][2 * i + 1], q);
      }
      const float dm = mean[r] - mb[r];
      const float var = fmaxf(wave_sum(q) * (1.0f / DM) - dm * dm, 0.f);
      const float sd1 = rbf(__builtin_amdgcn_sqrtf(rbf(rbf(var) + 1e-5f)));
      rs1[r] = __builtin_amdgcn_rcpf(sd1);
      const float mb2 = rbf(dm * rs1[r]);
      const float sd2 = rbf(__builtin_amdgcn_sqrtf(rbf(rbf(var * rs1[r] * rs1[r]) + 1e-5f)));
      if (lane == r) { myboa = -(mb[r] + mb2 * sd1); myinva = sd1 * sd2; }
    }
    st128s(v4u{asu(myboa), tlo, asu(myinva), tlo}, rW, lane < 8 ? lane * 16 : 0x70000000u, WS_ST + tok0 * 16);
    STAMP(2);
#pragma unroll
    for (int i = 0; i < 4; ++i) {
      unsigned o[8];
#pragma unroll
      for (int r = 0; r < 8; ++r) {
        unsigned t = pkbf(d[r][2 * i], d[r][2 * i + 1]);
        t = pkbf(bflo(t) * rs1[r], bfhi(t) * rs1[r]);
        o[r] = pkh(bflo(t), bfhi(t));
      }
      if (lane < 56) {
        const unsigned row = (lane * 4 + i) * 256;
        *lp<v4u>(row + (((2 * w) ^ (lane & 7)) << 4)) = v4u{o[0], o[1], o[2], o[3]};
        *lp<v4u>(row + (((2 * w + 1) ^ (lane & 7)) << 4)) = v4u{o[4], o[5], o[6], o[7]};
      }
    }
    BAR();
    STAMP(3);
#pragma unroll
    for (int k = 0; k < 7; ++k) {
      const unsigned sidx = k * 512 + tid, pair = sidx >> 4, pos = sidx & 15;
      v4u v = *lp<v4u>(sidx * 16);
      st128s(v, rW, pair * 8192 + ((pos ^ ((pair >> 2) & 7)) << 4), WS_XN + hid * 256);
    }
    asm volatile("s_waitcnt vmcnt(0)" : "+v"(tch) :: "memory");
    BAR();
    STAMP(4);
    if (tid == 0) st64s(v2u{tlo, thi}, rW, 0, WS_FX + hid * 8);
    if (hid == 0) {
      unsigned vo2 = tid < 224 ? WS_FH + ((tid / 14) * 4 * 256) + (tid % 14) * 16 : WS_FX + (tid & 31) * 8;
      spin64(v2u{tlo + 1, thi}, tid < 256, rW, vo2, tlo, thi);
      BAR();
      if (tid == 0) st64s(v2u{tlo, thi}, rW, 0, WS_E);
    }
    STAMP(5);
  } else {
    PROLOG_LOADS();
    const unsigned m = idx - 4, ma = m / 7, mb_ = m % 7;
    const unsigned tt = 4 * (xcd >> 1) + ma, sl = 7 * (xcd & 1) + mb_, tb = 4 * xcd + ma, cb = mb_, cp = 28 * xcd + m;
    const unsigned rot = (mb_ + 2 * ma) % 7;
    const unsigned tg = w & 3, hg = w >> 2, hf = lane >> 5, l31 = lane & 31, l15 = lane & 15, q4 = lane >> 4;
    unsigned plA, pAi, pCr, pCi, pb1a, pb1b;
    {
      const unsigned vo = tid * 2, so = cp * 1024;
      ALD_U16(plA, vo, mkrs(p.logA, DM * 512), so);
      ALD_U16(pAi, vo, mkrs(p.Aim, DM * 512), so);
      ALD_U16(pCr, vo, mkrs(p.Cr, DM * 512), so);
      ALD_U16(pCi, vo, mkrs(p.Ci, DM * 512), so);
      const rsrc_t rB1 = mkrs(p.b1, FF * 2);
      ALD_U16(pb1a, l31 * 2, rB1, sl * 256 + hg * 128);
      ALD_U16(pb1b, l31 * 2, rB1, sl * 256 + hg * 128 + 64);
    }
    const rsrc_t rW1 = mkrs(p.w1, DM * FF * 2);
    const unsigned vx0 = (lane >> 3) * 896 + (((lane & 7) ^ (lane >> 4)) << 4);
    const unsigned vx1 = (lane >> 3) * 896 + (((lane & 7) ^ (4 + (lane >> 4))) << 4) + 8 * 896;
    const unsigned vw1 = q4 * 3584 + ((l15 ^ (q4 << 2)) << 4);
    const unsigned vw2 = (lane >> 3) * 896 + (((lane & 7) ^ (((lane >> 4) & 1) << 2)) << 4);
    auto chunk_of = [&](int i) { unsigned c = i + rot; return c >= 7 ? c - 7 : c; };
    auto issueD = [&](int i) {
      const unsigned c = chunk_of(i), dst = (i & 3) * 32768 + w * 2048;
      const unsigned sx = (tt * 128 + w * 16) * 896 + c * 128, sw = (c * 64 + w * 8) * 3584 + sl * 256;
      dma16(rX, vx0, sx, dst); dma16(rX, vx1, sx, dst + 1024);
      dma16(rW1, vw1, sw, dst + 16384); dma16(rW1, vw1, sw + 4 * 3584, dst + 16384 + 1024);
    };
    auto wslot = [&](int i) { return (i % 3 == 0 ? 3u : (unsigned)(i % 3 - 1)) * 32768 + w * 4096; };
    auto issueW = [&](int i) {
      const unsigned s = ((w * 7 + chunk_of(i)) * 32) * 896 + cb * 128, dst = wslot(i);
#pragma unroll
      for (int j = 0; j < 4; ++j) dma16(rW2, vw2, s + j * 8 * 896, dst + j * 1024);
    };
    issueD(0); issueD(1);
    const unsigned ldtp = ((const CAS unsigned*)p.ldt)[cp], dskp = ((const CAS unsigned*)p.Dsk)[cp];
    const float dt0 = rbf(expf(bflo(ldtp))), dt1 = rbf(expf(bfhi(ldtp)));
    asm volatile("s_waitcnt vmcnt(8)" : "+v"(Ev), "+v"(plA), "+v"(pAi), "+v"(pCr), "+v"(pCi), "+v"(pb1a), "+v"(pb1b) :: "memory");
    STAMP(1);
    const u64 tag = (((u64)(unsigned)__builtin_amdgcn_readfirstlane(Ev.y) << 32) | (unsigned)__builtin_amdgcn_readfirstlane(Ev.x)) + 1;
    const unsigned tlo = (unsigned)tag, thi = (unsigned)(tag >> 32);

    float ysc0, ysc1;
    {
      const float dt = hg ? dt1 : dt0;
      const float l2 = -rbf(__builtin_amdgcn_exp2f(bflo(plA) * 1.4426950408889634f)) * dt * 1.4426950408889634f;
      const float th = bflo(pAi) * dt * 0.15915494309189535f;
      auto cexp = [&](float s) {
        const float mg = __builtin_amdgcn_exp2f(l2 * s), f = __builtin_amdgcn_fractf(th * s);
        return c32{mg * __builtin_amdgcn_cosf(f), mg * __builtin_amdgcn_sinf(f)};
      };
      unsigned ew = wave_maxu(max(pCr & 0x7fffu, pCi & 0x7fffu)) >> 7;
      ew = min(max(ew, 1u), 253u);
      const float inv = asf((254u - ew) << 23);
      const c32 w1c = cexp(1.f), w128 = cexp(128.f);
      c32 w8 = cexp(8.f); w8.i = -w8.i;
      const c32 w2c = cmul(w1c, w1c), w4c = cmul(w2c, w2c);
      const unsigned sb = SPARE + w * 4096;
      auto put = [&](int row, c32 v) { *lp<unsigned>(sb + row * 256 + ((lane * 4) ^ ((row & 7) << 5))) = pkh(v.r, v.i); };
      auto frag = [&](int s) { return *lp<v4u>(sb + l15 * 256 + (((4 * s + q4) ^ (2 * (l15 & 7))) << 4)); };
      v4u Bf[4];
      {
        const c32 w16 = cmul(w8, w8);
        c32 e = {1.f, 0.f}, o = w8;
#pragma unroll
        for (int j = 0; j < 8; ++j) {
          put(2 * j, e); put(2 * j + 1, o);
          if (j == 0) e = w16; else if (j < 7) e = cmul(e, w16);
          if (j < 7) o = cmul(o, w16);
        }
#pragma unroll
        for (int s = 0; s < 4; ++s) Bf[s] = frag(s);
      }
      f4 kc[3];
      c32 b0 = {bflo(pCr) * inv, bflo(pCi) * inv}, b1c = cmul(b0, w128), bs = cmul(b1c, w128);
      {
        c32 u = b0, v = b1c;
#pragma unroll
        for (int j = 0; j < 8; ++j) {
          put(j, u); put(8 + j, v);
          if (j < 7) { u = cmul(u, w1c); v = cmul(v, w1c); }
        }
        kc[0] = f4{0.f, 0.f, 0.f, 0.f};
#pragma unroll
        for (int s = 0; s < 4; ++s) kc[0] = mfma16h(frag(s), Bf[s], kc[0]);
      }
      SB(); issueD(2); SB();
      {
#pragma unroll
        for (int i = 0; i < 2; ++i) {
          c32 u = bs; bs = cmul(bs, w128);
#pragma unroll
          for (int j = 0; j < 4; ++j) { put(4 * i + j, u); if (j < 3) u = cmul(u, w2c); }
        }
#pragma unroll
        for (int i = 0; i < 4; ++i) { put(8 + 2 * i, bs); put(9 + 2 * i, cmul(bs, w4c)); bs = cmul(bs, w128); }
        kc[1] = f4{0.f, 0.f, 0.f, 0.f};
#pragma unroll
        for (int s = 0; s < 4; ++s) kc[1] = mfma16h(frag(s), Bf[s], kc[1]);
#pragma unroll
        for (int i = 0; i < 9; ++i) { put(i, bs); if (i < 8) bs = cmul(bs, w128); }
        kc[2] = f4{0.f, 0.f, 0.f, 0.f};
#pragma unroll
        for (int s = 0; s < 4; ++s) kc[2] = mfma16h(frag(s), Bf[s], kc[2]);
      }
      const float scw = asf(ew << 23);
#pragma unroll
      for (int t = 0; t < 3; ++t) {
        f4 v = kc[t] * scw;
        if (t == 2 && lane == 63) v.w = scw;
        *lp<f4>(sb + t * 1024 + lane * 16) = v;
      }
      STAMP(2);
      BAR();
      {
        unsigned e0 = 0, e1 = 0;
#pragma unroll
        for (int k = 0; k < 4; ++k) { e0 = max(e0, *lp<unsigned>(SPARE + k * 4096 + 3068)); e1 = max(e1, *lp<unsigned>(SPARE + (4 + k) * 4096 + 3068)); }
        e0 = __builtin_amdgcn_readfirstlane(e0); e1 = __builtin_amdgcn_readfirstlane(e1);
        ysc0 = 2.f * dt0 * asf(e0); ysc1 = 2.f * dt1 * asf(e1);
        if (tg < 3) {
          const float invc = asf((254u << 23) - (hg ? e1 : e0));
          const unsigned pa = SPARE + hg * 16384 + tg * 1024 + lane * 16;
          f4 v = (*lp<f4>(pa) + *lp<f4>(pa + 4096)) + (*lp<f4>(pa + 8192) + *lp<f4>(pa + 12288));
          v *= invc;
          const bool fine = tg == 0 || (tg == 1 && q4 < 2);
          const unsigned sh = tg == 0 ? 3u : tg == 2 ? 0u : (q4 < 2 ? 2u : 1u);
          const unsigned ia = (tg == 0 ? 128 * (q4 >> 1) + 4 * (q4 & 1) : 256 * tg + 64 * q4) + (l15 << sh) + 768 * hg;
          const unsigned s1 = fine ? 2u : 32u, s0 = tg == 2 ? 16u : 1u;
#pragma unroll
          for (int i = 0; i < 4; ++i) {
            const unsigned ix = ia + (i >> 1) * s1 + (i & 1) * s0;
            *lp<_Float16>(SPARE + 3072 + 2 * ix + 3072 * (ix >> 9)) = (_Float16)v[i];
          }
        }
      }
      BAR();
      {
        const unsigned t = tid & 255;
        unsigned v[9];
#pragma unroll
        for (int e = 0; e < 9; ++e) {
          const int lag = 2047 - 8 * (int)t - e;
          unsigned ix = lag < 256 ? (unsigned)max(lag, 0) : (pkbf((float)lag, 0.f) & 0xffffu) - 0x4300u + 128u;
          ix += 768 * hg;
          v[e] = *lp<u16>(SPARE + 3072 + 2 * ix + 3072 * (ix >> 9));
        }
        if (t == 255) v[8] = 0;
        const unsigned kb = KREV + hg * 2 * KCP + t * 16;
        *lp<v4u>(kb) = v4u{v[1] | v[2] << 16, v[3] | v[4] << 16, v[5] | v[6] << 16, v[7] | v[8] << 16};
        *lp<v4u>(kb + KCP) = v4u{v[0] | v[1] << 16, v[2] | v[3] << 16, v[4] | v[5] << 16, v[6] | v[7] << 16};
        if (tid < 16) *lp<v4u>(KREV + (tid >> 2) * KCP + 4096 + (tid & 3) * 16) = v4u{0, 0, 0, 0};
      }
    }
    SB(); issueD(3); SB();
    STAMP(3);

    f16v acc[2];
#pragma unroll
    for (int i = 0; i < 16; ++i) { acc[0][i] = 0.f; acc[1][i] = 0.f; }
    float cs0 = 0.f, cs1 = 0.f;
    v4u stg;
    {
      unsigned aB[4], aA[2][4];
      const unsigned rowB = tg * 32 + l31, qa = l15 >> 2, pp = lane & 3, blk = (lane >> 4) & 1;
#pragma unroll
      for (int i = 0; i < 4; ++i) {
        const unsigned ks = (i + tg) & 3;
        aB[i] = rowB * 128 + (((2 * ks + hf) ^ ((rowB >> 1) & 7)) << 4);
#pragma unroll
        for (int mm = 0; mm < 2; ++mm)
          aA[mm][i] = 16384 + (16 * ks + 8 * hf + qa) * 256 + (((4 * (2 * hg + mm) + 2 * blk + (pp >> 1)) ^ (qa << 2)) << 4) + 8 * (pp & 1);
      }
#pragma unroll
      for (int i = 0; i < 7; ++i) {
        if (i == 0) VMW(12); else if (i <= 4) VMW(8); else VMW(9);
        BAR();
        if (i == 0) STAMP(4);
        if (i >= 1 && i <= 3) issueD(i + 3);
        if (i == 4) ALD_B128S(stg, l31 * 16, rW, WS_ST + (tt * 128 + tg * 32) * 16);
        if (i >= 4) issueW(i - 4);
        const unsigned so = (i & 3) * 32768;
#pragma unroll
        for (int k = 0; k < 4; ++k) {
          const v4u B = *lp<v4u>(so + aB[k]);
          const v4u A0 = trfrag(so + aA[0][k], so + aA[0][k] + 1024), A1 = trfrag(so + aA[1][k], so + aA[1][k] + 1024);
          acc[0] = mfma32(A0, B, acc[0]); acc[1] = mfma32(A1, B, acc[1]);
          if (k == 0) {
#pragma unroll
            for (int j = 0; j < 4; ++j) { cs0 += bflo(A0[j]); cs0 += bfhi(A0[j]); cs1 += bflo(A1[j]); cs1 += bfhi(A1[j]); }
          }
        }
      }
    }
    *lp<f2>(SPARE + w * 512 + lane * 8) = f2{cs0, cs1};
    BAR();
    STAMP(5);
    float av;
    {
      const unsigned ca = SPARE + hg * 2048 + lane * 8;
      f2 c = (*lp<f2>(ca) + *lp<f2>(ca + 512)) + (*lp<f2>(ca + 1024) + *lp<f2>(ca + 1536));
      if (tid < 112) *lp<v4u>(UARR + (tid / 28) * UCP + (tid % 28) * 16) = v4u{0, 0, 0, 0};
      else if (tid < 114) *lp<unsigned>(UARR + (tid - 112) * 2 * UCP + 448) = 0;
      auto s0 = __builtin_amdgcn_permlane32_swap(asu(c.x), asu(c.x), false, false);
      auto s1 = __builtin_amdgcn_permlane32_swap(asu(c.y), asu(c.y), false, false);
      float t0 = asf(s0[0]) + asf(s0[1]), t1 = asf(s1[0]) + asf(s1[1]);
      asm volatile("" : "+v"(t0), "+v"(t1));
      const float A0 = hf ? bflo(pb1a) : t0, A1 = hf ? bflo(pb1b) : t1;
      asm volatile("s_waitcnt vmcnt(12)" : "+v"(stg) :: "memory");
#pragma unroll 1
      for (unsigned n = 0; n < SPIN_CAP; ++n) {
        if (__all(stg.y == tlo && stg.w == tlo)) break;
        __builtin_amdgcn_s_sleep(2);
        asm volatile("buffer_load_dwordx4 %0, %1, %2, %3 offen sc1\n\ts_waitcnt vmcnt(0)" : "=&v"(stg) : "v"(l31 * 16), "s"(rW), "s"(WS_ST + (tt * 128 + tg * 32) * 16) : "memory");
      }
      const float Bv = asf(hf ? stg.z : stg.x);
      av = __builtin_amdgcn_rcpf(asf(stg.z));
      acc[0] = __builtin_amdgcn_mfma_f32_32x32x2f32(A0, Bv, acc[0], 0, 0, 0);
      acc[1] = __builtin_amdgcn_mfma_f32_32x32x2f32(A1, Bv, acc[1], 0, 0, 0);
    }
    STAMP(6);
    v4u W[7][4];
    const unsigned aW0 = (4 * hf + (l15 >> 2)) * 128 + (((2 * ((lane >> 4) & 1) + ((lane & 3) >> 1)) ^ (((l15 >> 3) & 1) << 2)) << 4) + 8 * (lane & 1);
    auto readW = [&](int i) {
      const unsigned b = wslot(i);
#pragma unroll
      for (int s = 0; s < 2; ++s)
#pragma unroll
        for (int mc = 0; mc < 2; ++mc) { const unsigned a = b + s * 2048 + (aW0 ^ (mc * 64)); W[i][2 * s + mc] = trfrag(a, a + 1024); }
    };
    auto gelu_q = [&](int qq) {
      unsigned o[4];
#pragma unroll
      for (int j = 0; j < 4; ++j) {
        float r[2];
#pragma unroll
        for (int e = 0; e < 2; ++e) {
          const float v = acc[qq >> 1][8 * (qq & 1) + 2 * j + e] * av;
          const float z = v * __builtin_fmaf(v * v, -0.10294324f, -2.3022082f);
          r[e] = v * __builtin_amdgcn_rcpf(1.0f + __builtin_amdgcn_exp2f(z));
        }
        o[j] = pkbf(r[0], r[1]);
      }
      st128s(v4u{o[0], o[1], o[2], o[3]}, rW, lane * 16, WS_H + ((4 * tt + tg) * 112 + 8 * sl + 4 * hg + qq) * 1024);
    };
    v2u xf;
    v4u uv;
    gelu_q(0); VMW(9); readW(0); LGK0(); SB(); issueW(3); SB();
    gelu_q(1); ALD_B64S(xf, l31 * 8, rW, WS_FX); VMW(11); readW(1); LGK0(); SB(); issueW(4); SB();
    gelu_q(2); SB();
    gelu_q(3);
    STAMP(7);
    asm volatile("s_waitcnt vmcnt(6)" : "+v"(xf) :: "memory");
    spin64(xf, true, rW, WS_FX + l31 * 8, tlo, thi);
    ALD_B128S(uv, tid * 16, rW, WS_XN + cp * 8192);
    readW(2); readW(3); LGK0(); SB(); issueW(5); issueW(6); SB();
    STAMP(8);
    asm volatile("s_waitcnt vmcnt(8)" : "+v"(uv) :: "memory");
    if (lane == 0) st64s(v2u{tlo, thi}, rW, 0, WS_FH + (4 * tt + tg) * 256 + (2 * sl + hg) * 8);
    readW(4);
    {
      const unsigned a0 = __builtin_amdgcn_perm(uv.y, uv.x, 0x05040100), a1 = __builtin_amdgcn_perm(uv.w, uv.z, 0x05040100);
      const unsigned c0 = __builtin_amdgcn_perm(uv.y, uv.x, 0x07060302), c1 = __builtin_amdgcn_perm(uv.w, uv.z, 0x07060302);
      const unsigned ub = UARR + 448 + tid * 8;
      *lp<unsigned>(ub + 4) = a0; *lp<unsigned>(ub + 8) = a1;
      *lp<v2u>(ub + UCP) = v2u{a0, a1};
      *lp<unsigned>(ub + 2 * UCP + 4) = c0; *lp<unsigned>(ub + 2 * UCP + 8) = c1;
      *lp<v2u>(ub + 3 * UCP) = v2u{c0, c1};
    }
    BAR();
    STAMP(9);
    v2u hfl;
    {
      const unsigned jh = w & 1, thh = (w >> 1) & 1;
      f4 ya[4];
#pragma unroll
      for (int i = 0; i < 4; ++i) ya[i] = f4{0.f, 0.f, 0.f, 0.f};
      const unsigned fa = KREV + (hg * 2 + (l15 & 1)) * KCP + 2 * (2044 - 1792 - 128 * jh - 4 * (l15 >> 1) + 8 * q4);
      const unsigned ga = UARR + (hg * 2 + (l15 & 1)) * UCP + 2 * (224 - 128 * jh + 32 * (l15 >> 1) + 8 * q4);
      if (thh) conv_loop<1>(fa, ga, ya); else conv_loop<0>(fa, ga, ya);
      STAMP(10);
      ALD_B64S(hfl, lane * 8, rW, WS_FH + tb * 512);
      VMW(2); readW(5); readW(6);
      BAR();
      const unsigned yb = (hg * 2 + jh) * YCH + 4 * (36 * (l15 >> 1) + 2 * (l15 & 1) + 8 * q4);
#pragma unroll
      for (int i = 0; i < 4; ++i) {
        const unsigned t = thh ? (i == 0 ? 1 : i == 1 ? 2 : i == 2 ? 5 : 6) : (i == 0 ? 0 : i == 1 ? 3 : i == 2 ? 4 : 7);
        *lp<f2>(yb + t * 1152) = f2{ya[i].x, ya[i].y};
        *lp<f2>(yb + t * 1152 + 16) = f2{ya[i].z, ya[i].w};
      }
      BAR();
      const unsigned yr = tid * 16 + (tid >> 3) * 16;
      const f4 y0 = (*lp<f4>(yr) + *lp<f4>(yr + YCH)) * ysc0, y1 = (*lp<f4>(yr + 2 * YCH) + *lp<f4>(yr + 3 * YCH)) * ysc1;
      BAR();
      const float D0 = bflo(dskp), D1 = bfhi(dskp);
      v4u sv;
#pragma unroll
      for (int i = 0; i < 4; ++i) {
        const unsigned yy = pkbf(y0[i], y1[i]), ud = pkbf(hlo(uv[i]) * D0, hhi(uv[i]) * D1);
        sv[i] = pkbf(bflo(yy) + bflo(ud), bfhi(yy) + bfhi(ud));
      }
      st128s(sv, rW, tid * 16, WS_S + cp * 8192);
    }
    STAMP(11);
    {
      asm volatile("s_waitcnt vmcnt(1)" : "+v"(hfl) :: "memory");
      const bool need = l31 >= ((14 * w) >> 2) && l31 <= ((14 * w + 13) >> 2);
      spin64(hfl, need, rW, WS_FH + tb * 512 + lane * 8, tlo, thi);
    }
    STAMP(12);
    const unsigned etok = tb * 64 + (tid >> 3), ech = cb * 64 + (tid & 7) * 8;
    v4u H[14];
    auto loadH = [&](int half, int k) {
      return __builtin_amdgcn_raw_buffer_load_b128(rW, lane * 16, WS_H + ((2 * tb + half) * 112 + 14 * w + 2 * chunk_of(k >> 1) + (k & 1)) * 1024, 16);
    };
#pragma unroll
    for (int k = 0; k < 14; ++k) H[k] = loadH(0, k);
    SB();
    VMW(14);
    if (lane == 0) st64s(v2u{tlo, thi}, rW, 0, WS_FS + ((cp >> 5) * 8 + w) * 256 + (cp & 31) * 8);
    SB();
    const unsigned pw = w * 64 * PROW + l31 * PROW + hf * 16;
    v2u sfl;
    v4u xe, b2e;
    unsigned se[4];
#pragma unroll
    for (int half = 0; half < 2; ++half) {
      f16v o0, o1;
#pragma unroll
      for (int i = 0; i < 16; ++i) { o0[i] = 0.f; o1[i] = 0.f; }
#pragma unroll
      for (int k = 0; k < 14; ++k) {
        o0 = mfma32(W[k >> 1][2 * (k & 1)], H[k], o0);
        o1 = mfma32(W[k >> 1][2 * (k & 1) + 1], H[k], o1);
        if (half == 0) H[k] = loadH(1, k);
        SB();
      }
      if (half == 0) {
        xe = __builtin_amdgcn_raw_buffer_load_b128(rX, etok * 896 + ech * 2, 0, 0);
        b2e = __builtin_amdgcn_raw_buffer_load_b128(mkrs(p.b2, DM * 2), ech * 2, 0, 0);
        sfl = __builtin_bit_cast(v2u, __builtin_amdgcn_raw_buffer_load_b64(rW, l31 * 8, WS_FS + (cb * 8 + (tb >> 2)) * 256, 16));
        STAMP(13);
      } else {
        STAMP(14);
        spin64(sfl, true, rW, WS_FS + (cb * 8 + (tb >> 2)) * 256 + l31 * 8, tlo, thi);
#pragma unroll
        for (int i = 0; i < 4; ++i) se[i] = __builtin_amdgcn_raw_buffer_load_b32(rW, ((ech >> 1) + i) * 8192 + etok * 4, WS_S, 16);
      }
#pragma unroll
      for (int g = 0; g < 4; ++g) {
        *lp<f4>(pw + half * 32 * PROW + g * 32) = f4{o0[4 * g], o0[4 * g + 1], o0[4 * g + 2], o0[4 * g + 3]};
        *lp<f4>(pw + half * 32 * PROW + g * 32 + 128) = f4{o1[4 * g], o1[4 * g + 1], o1[4 * g + 2], o1[4 * g + 3]};
      }
      SB();
    }
    BAR();
    STAMP(15);
    {
      const unsigned pr = (tid >> 3) * PROW + (tid & 7) * 32;
      f4 a = *lp<f4>(pr), b = *lp<f4>(pr + 16);
#pragma unroll
      for (int k = 1; k < 8; ++k) { a += *lp<f4>(pr + k * 64 * PROW); b += *lp<f4>(pr + k * 64 * PROW + 16); }
      v4u ov;
#pragma unroll
      for (int i = 0; i < 4; ++i) {
        const float p0 = i < 2 ? a[2 * i] : b[2 * i - 4], p1 = i < 2 ? a[2 * i + 1] : b[2 * i - 3];
        unsigned t = pkbf(p0, p1);
        t = pkbf(bflo(t) + bflo(b2e[i]), bfhi(t) + bfhi(b2e[i]));
        const float o0 = bflo(t), o1 = bfhi(t);
        t = pkbf(o0 * __builtin_amdgcn_rcpf(1.0f + __builtin_amdgcn_exp2f(o0 * -1.4426950408889634f)),
                 o1 * __builtin_amdgcn_rcpf(1.0f + __builtin_amdgcn_exp2f(o1 * -1.4426950408889634f)));
        t = pkbf(bflo(t) * bflo(se[i]), bfhi(t) * bfhi(se[i]));
        ov[i] = pkbf(bflo(xe[i]) + bflo(t), bfhi(xe[i]) + bfhi(t));
      }
      __builtin_amdgcn_raw_buffer_store_b128(ov, mkrs(p.out, LT * DM * 2), etok * 896 + ech * 2, 0, 16);
    }
    STAMP(16);
    if (blockIdx.x == ENCB && w == 0) {
      u64 val = 0;
      u64 target = ert0 + 6000 + 4 * val;
      for (int n = 0; n < 100000; ++n) { if (__builtin_amdgcn_s_memrealtime() >= target) break; __builtin_amdgcn_s_sleep(1); }
    }
  }
}

extern "C" void kernel_launch(void* const* d_in, const int* in_sizes, int n_in, void* d_out, int out_size, void* d_ws, size_t ws_size,
                              hipStream_t stream) {
  Params p;
  p.x = (const u16*)d_in[0]; p.logA = (const u16*)d_in[1]; p.Aim = (const u16*)d_in[2]; p.Cr = (const u16*)d_in[3];
  p.Ci = (const u16*)d_in[4]; p.ldt = (const u16*)d_in[5]; p.Dsk = (const u16*)d_in[6]; p.w1 = (const u16*)d_in[8];
  p.b1 = (const u16*)d_in[9]; p.w2 = (const u16*)d_in[10]; p.b2 = (const u16*)d_in[11];
  p.out = (u16*)d_out; p.ws = (char*)d_ws;
  s4d_mlp_gate_kernel<<<256, 512, 0, stream>>>(p);
}
```

```cpp
#define ENC 99
#define ENCB 100
#define ENCW 4
#include <hip/hip_runtime.h>
#include <hip/hip_bf16.h>

typedef unsigned short u16;
typedef unsigned long long u64;
typedef int v4i __attribute__((ext_vector_type(4)));
typedef unsigned v4u __attribute__((ext_vector_type(4)));
typedef unsigned v2u __attribute__((ext_vector_type(2)));
typedef float f2 __attribute__((ext_vector_type(2)));
typedef float f4 __attribute__((ext_vector_type(4)));
typedef float f16v __attribute__((ext_vector_type(16)));
typedef short s4 __attribute__((ext_vector_type(4)));
typedef unsigned short us2 __attribute__((ext_vector_type(2)));
typedef __bf16 b8 __attribute__((ext_vector_type(8)));
typedef __bf16 b2 __attribute__((ext_vector_type(2)));
typedef _Float16 h8 __attribute__((ext_vector_type(8)));
typedef _Float16 h2 __attribute__((ext_vector_type(2)));
#define LAS __attribute__((address_space(3)))
#define DEV __device__ __forceinline__
#define SB() __builtin_amdgcn_sched_barrier(0)
#define VMW(n) asm volatile("s_waitcnt vmcnt(" #n ")" ::: "memory")
#define LGK0() asm volatile("s_waitcnt lgkmcnt(0)" ::: "memory")
#define BAR() asm volatile("s_waitcnt lgkmcnt(0)\n\ts_barrier" ::: "memory")
#define OPQ(x) asm volatile("" : "+v"(x))
#define OPQS(x) asm volatile("" : "+s"(x))

#define STAMPS 0
#define STAMP(i) do { if (i == ENC) { SB(); asm volatile("s_memrealtime %0\n\ts_waitcnt lgkmcnt(0)" : "=s"(ert), "+s"(ert0) :: "memory"); SB(); } } while (0)

constexpr int LT = 2048, DM = 448, FF = 1792;
constexpr unsigned WS_E = 0, WS_FX = 4096, WS_ST = 32768, WS_FH = 65536, WS_FS = WS_FH + 64 * 4096, WS_XN = WS_FS + 56 * 4096,
                   WS_S = WS_XN + 224 * 8192, WS_H = WS_S + 224 * 8192, WS_END = WS_H + 64 * 112 * 1024, WS_DBG = 16u << 20;
constexpr unsigned KCP = 4224, KROW = 288, KSCR = 16 * KROW, TTAB = 28672, UARR = 32768, UCP = 4640, CSX = 24576, YSC = 31744, YBUF = 65536, SBUF = 131072;
constexpr unsigned slot_of(int i) { return 32768u + ((i + 2) & 3) * 32768u; }
constexpr unsigned wslot_of(int i) { return 65536u + 32768u * (i % 3); }
#define HPF 0
#define HST 44
#define KEARLY 0
constexpr unsigned CODE_PF_BYTES = 19456;
constexpr unsigned SPIN_CAP = 1u << 14;
constexpr unsigned OOB = 0x70000000u;

struct Buf { const void* p; unsigned bytes, flags; };
struct HostParams { Buf b[13]; };
struct Params { v4i lA, Ai, Cr, Ci, ws, x, w1, b1, ldt, w2, Dsk, b2, out; };

__device__ v4u ld128(v4i rs, unsigned vo, unsigned so, int aux) __asm("llvm.amdgcn.raw.buffer.load.v4i32");
__device__ v2u ld64(v4i rs, unsigned vo, unsigned so, int aux) __asm("llvm.amdgcn.raw.buffer.load.v2i32");
__device__ unsigned ld32(v4i rs, unsigned vo, unsigned so, int aux) __asm("llvm.amdgcn.raw.buffer.load.i32");
__device__ void st128(v4u v, v4i rs, unsigned vo, unsigned so, int aux) __asm("llvm.amdgcn.raw.buffer.store.v4i32");
__device__ void st64(v2u v, v4i rs, unsigned vo, unsigned so, int aux) __asm("llvm.amdgcn.raw.buffer.store.v2i32");

DEV float asf(unsigned u) { return __builtin_bit_cast(float, u); }
DEV unsigned asu(float f) { return __builtin_bit_cast(unsigned, f); }
DEV float bflo(unsigned u) { return asf(u << 16); }
DEV float bfhi(unsigned u) { return asf(u & 0xffff0000u); }
DEV f2 bf2(unsigned u) { return f2{bflo(u), bfhi(u)}; }
DEV unsigned pkbf(f2 v) { return __builtin_bit_cast(unsigned, __builtin_convertvector(v, b2)); }
DEV unsigned pkh(f2 v) { return __builtin_bit_cast(unsigned, __builtin_convertvector(v, h2)); }
DEV float rbf(float a) { return bflo(pkbf(f2{a, a})); }
DEV f2 h2f(unsigned u) { h2 h = __builtin_bit_cast(h2, u); return f2{(float)h.x, (float)h.y}; }
DEV f2 fma2(f2 a, f2 b, f2 c) { return __builtin_elementwise_fma(a, b, c); }
DEV float dot2(unsigned a, unsigned b, float s) { return __builtin_amdgcn_fdot2_f32_bf16(__builtin_bit_cast(b2, a), __builtin_bit_cast(b2, b), s, false); }
template <typename T> DEV LAS T* lp(unsigned a) { return (LAS T*)(size_t)a; }
#define ALD_U16(d, vo, rs, so) asm volatile("buffer_load_ushort %0, %1, %2, %3 offen" : "=v"(d) : "v"(vo), "s"(rs), "s"(so) : "memory")
#define ALD_B64(d, vo, rs, so) asm volatile("buffer_load_dwordx2 %0, %1, %2, %3 offen" : "=v"(d) : "v"(vo), "s"(rs), "s"(so) : "memory")
#define ALD_B64S(d, vo, rs, so) asm volatile("buffer_load_dwordx2 %0, %1, %2, %3 offen sc1" : "=v"(d) : "v"(vo), "s"(rs), "s"(so) : "memory")
#define ALD_B128S(d, vo, rs, so) asm volatile("buffer_load_dwordx4 %0, %1, %2, %3 offen sc1" : "=v"(d) : "v"(vo), "s"(rs), "s"(so) : "memory")
DEV v4u trfrag(unsigned a) {
  s4 lo = __builtin_amdgcn_ds_read_tr16_b64_v4i16(lp<s4>(a)), hi = __builtin_amdgcn_ds_read_tr16_b64_v4i16(lp<s4>(a + 1024));
  v2u l = __builtin_bit_cast(v2u, lo), h = __builtin_bit_cast(v2u, hi);
  return v4u{l.x, l.y, h.x, h.y};
}
DEV f16v mfma32(v4u a, v4u b, f16v c) { return __builtin_amdgcn_mfma_f32_32x32x16_bf16(__builtin_bit_cast(b8, a), __builtin_bit_cast(b8, b), c, 0, 0, 0); }
DEV f4 mfma16h(v4u a, v4u b, f4 c) { return __builtin_amdgcn_mfma_f32_16x16x32_f16(__builtin_bit_cast(h8, a), __builtin_bit_cast(h8, b), c, 0, 0, 0); }
template <int C> DEV float dppf(float v) { return asf(__builtin_amdgcn_update_dpp(0, asu(v), C, 0xf, 0xf, true)); }
DEV float sum8(float v) { v += dppf<0xB1>(v); v += dppf<0x4E>(v); v += dppf<0x141>(v); return v; }
template <int C, int RM> DEV unsigned dppu(unsigned v) { return __builtin_amdgcn_update_dpp(0, v, C, RM, 0xf, true); }
DEV unsigned wave_maxu(unsigned v) {
  v = max(v, dppu<0xB1, 0xf>(v)); v = max(v, dppu<0x4E, 0xf>(v)); v = max(v, dppu<0x141, 0xf>(v)); v = max(v, dppu<0x140, 0xf>(v));
  v = max(v, dppu<0x142, 0xa>(v)); v = max(v, dppu<0x143, 0xc>(v));
  return __builtin_amdgcn_readlane(v, 63);
}
DEV u64 as64(v2u v) { return __builtin_bit_cast(u64, v); }
DEV void await64(v2u f, v4i rs, unsigned vo, unsigned so, u64 tag) {
  if (__builtin_expect(__builtin_amdgcn_ballot_w64(as64(f) != tag) != 0, 0)) {
#pragma unroll 1
    for (unsigned n = 0; n < SPIN_CAP; ++n) {
      asm volatile("buffer_load_dwordx2 %0, %1, %2, %3 offen sc1\n\ts_waitcnt vmcnt(0)" : "=&v"(f) : "v"(vo), "s"(rs), "s"(so) : "memory");
      if (__builtin_amdgcn_ballot_w64(as64(f) != tag) == 0) break;
      __builtin_amdgcn_s_sleep(1);
    }
  }
}
struct cm { f2 w, r; };
DEV cm mkcm(f2 w) { return cm{w, f2{-w.y, w.x}}; }
DEV f2 cmul(f2 a, cm m) { return fma2(a.xx, m.w, a.yy * m.r); }
template <int N> DEV void kput(const unsigned (&v)[16]) {
  asm volatile("ds_write_addtid_b32 %0 offset:0\n\tds_write_addtid_b32 %1 offset:288\n\tds_write_addtid_b32 %2 offset:576\n\tds_write_addtid_b32 %3 offset:864\n\t"
               "ds_write_addtid_b32 %4 offset:1152\n\tds_write_addtid_b32 %5 offset:1440\n\tds_write_addtid_b32 %6 offset:1728\n\tds_write_addtid_b32 %7 offset:2016\n\t"
               "ds_write_addtid_b32 %8 offset:2304"
               :: "v"(v[0]), "v"(v[1]), "v"(v[2]), "v"(v[3]), "v"(v[4]), "v"(v[5]), "v"(v[6]), "v"(v[7]), "v"(v[8]) : "memory");
  if (N > 9)
    asm volatile("ds_write_addtid_b32 %0 offset:2592\n\tds_write_addtid_b32 %1 offset:2880\n\tds_write_addtid_b32 %2 offset:3168\n\tds_write_addtid_b32 %3 offset:3456\n\t"
                 "ds_write_addtid_b32 %4 offset:3744\n\tds_write_addtid_b32 %5 offset:4032\n\tds_write_addtid_b32 %6 offset:4320"
                 :: "v"(v[9]), "v"(v[10]), "v"(v[11]), "v"(v[12]), "v"(v[13]), "v"(v[14]), "v"(v[15]) : "memory");
}
static_assert(KROW == 288, "offsets above");
#define SETM0(x) asm volatile("s_mov_b32 m0, %0\n\ts_nop 0" :: "s"(x) : "memory")
#define DMA(vo, rs, so, imm) asm volatile("buffer_load_dwordx4 %0, %1, %2 offen offset:" #imm " lds" :: "v"(vo), "s"(rs), "s"(so) : "memory")

template <int TH> struct ConvPlan {
  static constexpr int PI[18] = {0, 0, 0, 0, 1, 1, 1, TH ? 1 : 2, 2, 2, TH ? 2 : 3, 3, 3, 4, 4, 5, TH ? 5 : 6, TH ? 6 : 7};
  static constexpr int PE0[18] = {0, 3, 4, 7, 3, 2, 6, 2, 1, 5, 0, 1, 4, 0, 3, 2, 1, 0};
  static constexpr int PE1[18] = {1, 2, 5, 6, 5, 1, 0, 4, 4, 0, 3, 3, 2, 2, 1, 1, 0, 0};
  static constexpr int pe(int m) { return TH ? PE1[m] : PE0[m]; }
  static constexpr int acc(int m) { const int t = PI[m] + pe(m); return TH ? (t == 1 ? 0 : t == 2 ? 1 : t == 5 ? 2 : 3) : (t == 0 ? 0 : t == 3 ? 1 : t == 4 ? 2 : 3); }
  static constexpr bool newF(int m) { return m == 0 || PI[m] != PI[m - 1]; }
  static constexpr bool newG(int m) { for (int k = 0; k < m; ++k) if (pe(k) == pe(m)) return false; return true; }
};
template <int V> struct IC { static constexpr int v = V; };
template <int B, int E, typename FN> DEV void sfor(FN&& f) { if constexpr (B < E) { f(IC<B>{}); sfor<B + 1, E>(f); } }
template <int TH, typename FN> DEV void conv_all(unsigned fa, unsigned fb, unsigned ga, f4 (&acc)[4], FN&& hook) {
  using P = ConvPlan<TH>;
  constexpr int LA = 6;
  v4u Fv[2][8], Gv[2][8];
  auto fetch = [&](auto M) {
    constexpr int jj = M.v / 18, pm = M.v % 18, i = P::PI[pm], e = P::pe(pm);
    if constexpr (P::newF(pm)) {
      v2u a = *lp<v2u>(fa + 512 * (7 - i) + 64 * (3 - jj)), b = *lp<v2u>(fb + 512 * (7 - i) + 64 * (3 - jj));
      Fv[jj & 1][i] = v4u{a.x, a.y, b.x, b.y};
    }
    if constexpr (P::newG(pm)) Gv[jj & 1][e] = *lp<v4u>(ga + 512 * e + 64 * (3 - jj));
  };
  sfor<0, LA>([&](auto M) { fetch(M); SB(); });
  sfor<0, 72>([&](auto M) {
    constexpr int jj = M.v / 18, pm = M.v % 18, i = P::PI[pm], e = P::pe(pm), t = P::acc(pm);
    acc[t] = mfma16h(Fv[jj & 1][i], Gv[jj & 1][e], acc[t]);
    SB();
    if constexpr (M.v + LA < 72) fetch(IC<M.v + LA>{});
    hook(M);
    SB();
  });
}

__global__ __launch_bounds__(512, 2) void s4d_mlp_gate_kernel(Params p) {
  __shared__ __attribute__((aligned(16))) char smem[163840];
  asm volatile("" :: "v"((LAS char*)smem) : "memory");
  const unsigned tid = threadIdx.x, lane = tid & 63;
  const unsigned w = __builtin_amdgcn_readfirstlane(tid >> 6);
  const unsigned xcd = blockIdx.x & 7, idx = blockIdx.x >> 3;
  u64 ert0, ert = 0;
  asm volatile("s_memrealtime %0" : "=s"(ert0) :: "memory");
  {
    u64 pc; asm volatile("s_getpc_b64 %0" : "=s"(pc));
    const v4i rC = {(int)(unsigned)pc, (int)(unsigned)(pc >> 32), (int)CODE_PF_BYTES, 0x00020000};
    asm volatile("s_mov_b32 m0, %0\n\ts_nop 0\n\tbuffer_load_dword %1, %2, %3 offen lds" :: "s"(slot_of(3) + w * 4096),
                 "v"(lane < 4 ? lane * 128 : OOB), "s"(rC), "s"(((blockIdx.x & 56) + w) * 512) : "memory");
    SB();
  }
  if (idx < 4) {
    const unsigned hid = xcd * 4 + idx, tok0 = hid * 64 + w * 8, r8 = lane >> 3, part = lane & 7;
    v2u Ev;
    unsigned vo = r8 * 896 + part * 16, so = tok0 * 896, zero = 0;
    OPQ(vo); OPQS(so); OPQ(zero); SB();
    v4u xr[7];
#pragma unroll
    for (int k = 0; k < 7; ++k) xr[k] = ld128(p.x, vo + k * 128, so, 0);
    ALD_B64S(Ev, zero, p.ws, 0u);
    unsigned vt = lane < 56 ? lane * 128 : OOB, st0 = (w * 7 * 32 + idx * 8) * 896;
    OPQ(vt); OPQS(st0);
    SETM0(65536 + w * 256);
    auto touch = [&](v4i rs, unsigned vo, unsigned so) { asm volatile("buffer_load_dword %0, %1, %2 offen lds" :: "v"(vo), "s"(rs), "s"(so) : "memory"); };
    float s0 = 0.f, s1 = 0.f, q0 = 0.f, q1 = 0.f;
#pragma unroll
    for (int k = 0; k < 7; ++k) {
      s0 = dot2(xr[k].x, 0x3f803f80u, s0); q0 = dot2(xr[k].x, xr[k].x, q0); s1 = dot2(xr[k].y, 0x3f803f80u, s1); q1 = dot2(xr[k].y, xr[k].y, q1);
      s0 = dot2(xr[k].z, 0x3f803f80u, s0); q0 = dot2(xr[k].z, xr[k].z, q0); s1 = dot2(xr[k].w, 0x3f803f80u, s1); q1 = dot2(xr[k].w, xr[k].w, q1);
    }
    OPQ(s0); OPQ(s1); OPQ(q0); OPQ(q1);
    touch(p.w2, vt, st0); touch(p.w2, vt, st0 + 1 * 32 * 896);
    STAMP(1);
    const float mean = sum8(s0 + s1) * (1.0f / DM);
    const float var = fmaxf(__builtin_fmaf(-mean, mean, sum8(q0 + q1) * (1.0f / DM)), 0.f);
    const float mb = rbf(mean);
    const float sd1 = rbf(__builtin_amdgcn_sqrtf(rbf(rbf(var) + 1e-5f)));
    const float rs1 = __builtin_amdgcn_rcpf(sd1);
    const float mb2 = rbf((mean - mb) * rs1);
    const float sd2 = rbf(__builtin_amdgcn_sqrtf(rbf(rbf(var * rs1 * rs1) + 1e-5f)));
    asm volatile("s_waitcnt vmcnt(2)" : "+v"(Ev) :: "memory");
    const u64 tag = (((u64)(unsigned)__builtin_amdgcn_readfirstlane(Ev.y) << 32) | (unsigned)__builtin_amdgcn_readfirstlane(Ev.x)) + 1;
    const v2u tg2 = {(unsigned)tag, (unsigned)(tag >> 32)};
    st128(v4u{asu(-__builtin_fmaf(mb2, sd1, mb)), tg2.x, asu(sd1 * sd2), tg2.x}, p.ws, part == 0 ? WS_ST + (tok0 + r8) * 16 : OOB, 0, 16);
    STAMP(2);
    {
      unsigned la = part * 1024 + ((((2 * w + (r8 >> 2)) ^ part) << 4) | ((r8 & 3) << 2));
      OPQ(la);
      const f2 rs2 = {rs1, rs1}, off2 = {-mb * rs1, -mb * rs1};
#pragma unroll
      for (int k = 0; k < 7; ++k)
#pragma unroll
        for (int i = 0; i < 4; ++i) *lp<unsigned>(la + k * 8192 + i * 256) = pkh(fma2(bf2(xr[k][i]), rs2, off2));
    }
    BAR();
    STAMP(3);
    {
      v4u v[7];
#pragma unroll
      for (int k = 0; k < 7; ++k) v[k] = *lp<v4u>(tid * 16 + k * 8192);
      const unsigned go = WS_XN + hid * 256 + (tid >> 4) * 8192 + (((tid & 15) ^ (w & 7)) << 4);
#pragma unroll
      for (int k = 0; k < 7; ++k) st128(v[k], p.ws, go + k * 32 * 8192, 0, 16);
    }
    touch(p.w2, vt, st0 + 2 * 32 * 896);
    VMW(1);
    BAR();
    STAMP(4);
    if (tid == 0) st64(tg2, p.ws, 0, WS_FX + hid * 128, 16);
    touch(p.w2, vt, st0 + 3 * 32 * 896); touch(p.w2, vt, st0 + 4 * 32 * 896); touch(p.w2, vt, st0 + 5 * 32 * 896); touch(p.w2, vt, st0 + 6 * 32 * 896);
    STAMP(6);
    STAMP(7);
    if (hid == 0) {
      const unsigned vo2 = tid < 224 ? WS_FH + ((tid / 14) * 4 * 4096) + (tid % 14) * 256 : WS_FX + (tid & 31) * 128;
      if (w < 4) await64(v2u{tg2.x + 1, tg2.y}, p.ws, vo2, 0u, tag);
      BAR();
      if (tid == 0) st64(tg2, p.ws, 0, WS_E, 16);
    }
    STAMP(5);
  } else {
    const unsigned ma = idx & 3, mb_ = (idx >> 2) - 1;
    const unsigned tt = 4 * (xcd >> 1) + ma, sl = 7 * (xcd & 1) + mb_, tb = 4 * xcd + ma, cb = mb_, cp = 28 * xcd + idx - 4;
        const bool rot = (ma + mb_) & 1;
    const unsigned tg = w & 3, hg = w >> 2, hf = lane >> 5, l31 = lane & 31, l15 = lane & 15, q4 = lane >> 4;
    unsigned vx0 = (lane >> 3) * 896 + (((lane & 7) ^ q4) << 4);
    unsigned vx1 = (vx0 ^ 64) + 8 * 896 - 1024;
    unsigned vw0 = q4 * 3584 + ((l15 ^ (q4 << 2)) << 4) + 2048;
    unsigned vw1 = vw0 + 4 * 3584 - 1024;
    unsigned sxA = (tt * 128 + w * 16) * 896 + (rot ? 4 * 128 : 0), swA = w * 8 * 3584 + sl * 256 + (rot ? 4 * 64 * 3584 : 0);
    unsigned vp = tid * 2, sp = cp * 1024, vc = lane * 8, sc = sp + hg * 512, vb = l31 * 2, sb1 = sl * 256 + hg * 128, zero = 0, sdt = cp * 4 + hg * 2;
    unsigned m0w = w * 4096; OPQS(m0w);
    OPQ(vx0); OPQ(vx1); OPQ(vw0); OPQ(vw1); OPQ(vp); OPQ(vc); OPQ(vb); OPQ(zero);
    OPQS(sxA); OPQS(swA); OPQS(sp); OPQS(sc); OPQS(sb1); OPQS(sdt);
    SB();
    unsigned plA, pAi, pCr, pCi, pb1a, pb1b, pdt;
    v2u Ev, mCr, mCi;
    ALD_U16(pdt, zero, p.ldt, sdt);
    ALD_U16(plA, vp, p.lA, sp);
    ALD_U16(pAi, vp, p.Ai, sp);
    ALD_U16(pCr, vp, p.Cr, sp);
    ALD_U16(pCi, vp, p.Ci, sp);
    ALD_B64(mCr, vc, p.Cr, sc);
    ALD_B64(mCi, vc, p.Ci, sc);
    v4i rW1 = p.w1;
    { const u64 b = (((u64)(unsigned)rW1.y << 32) | (unsigned)rW1.x) - 4096; rW1.x = (int)(unsigned)b; rW1.y = (int)(unsigned)(b >> 32); }
    unsigned sxB, swB;
    auto dmaD = [&](int i, int j) {
      const unsigned sx = (i < 3 ? sxA : sxB) + i * 128, sw = (i < 3 ? swA : swB) + i * (64 * 3584);
      if (j == 0) DMA(vx0, p.x, sx, 0); else if (j == 1) DMA(vx1, p.x, sx, 1024);
      else if (j == 2) DMA(vw0, rW1, sw, 2048); else DMA(vw1, rW1, sw, 3072);
    };
    SETM0(m0w + slot_of(0));
    asm volatile("s_barrier" ::: "memory");
    dmaD(0, 0); dmaD(0, 1); dmaD(0, 2); dmaD(0, 3);
    ALD_B64S(Ev, zero, p.ws, 0u);
    ALD_U16(pb1a, vb, p.b1, sb1);
    asm volatile("buffer_load_ushort %0, %1, %2, %3 offen offset:64" : "=v"(pb1b) : "v"(vb), "s"(p.b1), "s"(sb1) : "memory");
    SB();
    unsigned aB[2][4], aA[2][2][4];
    {
      const unsigned qa = l15 >> 2, pp = lane & 3, blk = q4 & 1;
      const unsigned b0 = 32768 + (2 * tg + (l31 >> 4)) * 4096 + l15 * 128, fx = (hf ^ ((l31 >> 1) & 7)) << 4;
      const unsigned a0 = 32768 + 2048 + hf * 4096 + qa * 256 + (((8 * hg + 2 * blk + (pp >> 1)) ^ (qa << 2)) << 4) + 8 * (pp & 1);
#pragma unroll
      for (int i = 0; i < 4; ++i) {
        const unsigned ks = (i + tg) & 3;
        aB[0][i] = b0 + (fx ^ (ks << 5)); aB[1][i] = aB[0][i] + 65536;
        aA[0][0][i] = a0 + ks * 8192; aA[0][1][i] = aA[0][0][i] ^ 64; aA[1][0][i] = aA[0][0][i] + 65536; aA[1][1][i] = aA[0][1][i] + 65536;
        OPQ(aB[0][i]); OPQ(aB[1][i]); OPQ(aA[0][0][i]); OPQ(aA[0][1][i]); OPQ(aA[1][0][i]); OPQ(aA[1][1][i]);
        SB(); if (i == 0) SETM0(m0w + slot_of(1)); dmaD(1, i); SB();
      }
    }
    unsigned km0 = ((w + 7) & 7) * KSCR; OPQS(km0);
    unsigned kfa = km0 + l15 * KROW + q4 * 16;
    OPQ(kfa);
    sxB = sxA - (rot ? 7 * 128 : 0); swB = swA - (rot ? 7 * 64 * 3584 : 0); OPQS(sxB); OPQS(swB);
    unsigned vw2 = (lane >> 3) * 896 + (((lane & 7) ^ ((q4 & 1) << 2)) << 4), vst = l31 * 16, sst = WS_ST + (tt * 128 + tg * 32) * 16;
    unsigned sv0 = w * 7 * 32 * 896 + cb * 128;
    OPQ(vw2); OPQ(vst); OPQS(sst); OPQS(sv0);
    auto dmaW = [&](int i, int j) {
      const unsigned s = sv0 + i * (32 * 896);
      if (j == 0) { SETM0(m0w + wslot_of(i)); DMA(vw2, p.w2, s, 0); } else if (j == 1) DMA(vw2, p.w2, s + 6144, 1024);
      else if (j == 2) DMA(vw2, p.w2, s + 12288, 2048); else DMA(vw2, p.w2, s + 18432, 3072);
    };
    v4u W[7][4];
    unsigned aW[2][2];
    aW[0][0] = 65536 + w * 4096 + (4 * hf + (l15 >> 2)) * 128 + (((2 * (q4 & 1) + ((lane & 3) >> 1)) ^ (((l15 >> 3) & 1) << 2)) << 4) + 8 * (lane & 1);
    aW[0][1] = aW[0][0] ^ 64;
    OPQ(aW[0][0]); OPQ(aW[0][1]);
    auto readW1 = [&](int i, int f) { W[i][f] = trfrag(aW[wslot_of(i) == 131072][f & 1] + (wslot_of(i) == 98304 ? 32768 : 0) + (f >> 1) * 2048); };
    auto readW = [&](int i) { readW1(i, 0); readW1(i, 1); readW1(i, 2); readW1(i, 3); };
    SB();
    asm volatile("s_waitcnt vmcnt(11)" : "+v"(pdt), "+v"(plA), "+v"(pAi), "+v"(pCr), "+v"(pCi), "+v"(mCr), "+v"(mCi) :: "memory");
    STAMP(1);

    f4 kc[3];
    float ysc;
    v4u Bf[4], Af[4];
    auto frags = [&](v4u (&f)[4]) {
#pragma unroll
      for (int s = 0; s < 4; ++s) f[s] = *lp<v4u>(kfa + s * 64);
    };
    auto kmm = [&](int t) {
      kc[t] = f4{0.f, 0.f, 0.f, 0.f};
#pragma unroll
      for (int s = 0; s < 4; ++s) kc[t] = mfma16h(Af[s], Bf[s], kc[t]);
    };
    {
      constexpr float L2E = 1.4426950408889634f;
      const float xd = bflo(pdt), td = xd * L2E;
      const float ed = __builtin_amdgcn_exp2f(td), rd = __builtin_fmaf(xd, 1.925963033500011e-8f, __builtin_fmaf(xd, L2E, -td));
      const float dt = rbf(__builtin_fmaf(ed, rd * 0.6931471805599453f, ed));
      const float l2 = -rbf(__builtin_amdgcn_exp2f(bflo(plA) * L2E)) * dt * L2E;
      const float th = bflo(pAi) * dt * 0.15915494309189535f;
      auto cexp = [&](float s) {
        const float mg = __builtin_amdgcn_exp2f(l2 * s), f = __builtin_amdgcn_fractf(th * s);
        return f2{__builtin_amdgcn_cosf(f), __builtin_amdgcn_sinf(f)} * mg;
      };
      us2 mx = __builtin_elementwise_max(__builtin_elementwise_max(__builtin_bit_cast(us2, mCr.x & 0x7fff7fffu), __builtin_bit_cast(us2, mCr.y & 0x7fff7fffu)),
                                         __builtin_elementwise_max(__builtin_bit_cast(us2, mCi.x & 0x7fff7fffu), __builtin_bit_cast(us2, mCi.y & 0x7fff7fffu)));
      unsigned ew = wave_maxu(max((unsigned)mx.x, (unsigned)mx.y)) >> 7;
      ew = ew < 1u ? 1u : ew > 253u ? 253u : ew;
      ysc = 2.f * dt * asf(ew << 23);
      unsigned tL[16], t0[16], t1[16], t2[16];
      {
        f2 w8 = cexp(8.f); w8.y = -w8.y;
        const f2 w16 = cmul(w8, mkcm(w8));
        const cm m16 = mkcm(w16);
        f2 e = w16, o = w8;
        tL[0] = 0x3c00u; tL[1] = pkh(o);
#pragma unroll
        for (int j = 1; j < 8; ++j) { o = cmul(o, m16); tL[2 * j] = pkh(e); tL[2 * j + 1] = pkh(o); if (j < 7) e = cmul(e, m16); }
      }
      const f2 w1v = cexp(1.f);
      const cm m1 = mkcm(w1v), m128 = mkcm(cexp(128.f));
      f2 u = f2{bflo(pCr), bflo(pCi)} * asf((254u - ew) << 23), v = cmul(u, m128), bs = cmul(v, m128);
#pragma unroll
      for (int j = 0; j < 8; ++j) { t0[j] = pkh(u); t0[8 + j] = pkh(v); if (j < 7) { u = cmul(u, m1); v = cmul(v, m1); } }
#pragma unroll
      for (int j = 0; j < 16; ++j) { OPQ(tL[j]); OPQ(t0[j]); }
      STAMP(8);
      SETM0(km0);
      kput<16>(tL); frags(Bf);
      SB();
      const f2 w2v = cmul(w1v, m1);
      const cm m2 = mkcm(w2v), m4 = mkcm(cmul(w2v, m2));
      u = bs; v = cmul(bs, m128); bs = cmul(v, m128);
#pragma unroll
      for (int j = 0; j < 4; ++j) { t1[j] = pkh(u); t1[4 + j] = pkh(v); if (j < 3) { u = cmul(u, m2); v = cmul(v, m2); } }
#pragma unroll
      for (int j = 0; j < 8; ++j) OPQ(t1[j]);
      kput<16>(t0); frags(Af);
      SB();
#pragma unroll
      for (int j = 0; j < 4; ++j) { t1[8 + 2 * j] = pkh(bs); t1[9 + 2 * j] = pkh(cmul(bs, m4)); bs = cmul(bs, m128); }
#pragma unroll
      for (int j = 8; j < 16; ++j) OPQ(t1[j]);
      SB();
      kmm(0);
      STAMP(20);
      kput<16>(t1); frags(Af);
      SB();
#pragma unroll
      for (int j = 0; j < 9; ++j) { t2[j] = pkh(bs); if (j < 8) bs = cmul(bs, m128); }
#pragma unroll
      for (int j = 0; j < 9; ++j) OPQ(t2[j]);
      SB();
      kmm(1);
      kput<9>(t2); frags(Af);
    }
    STAMP(2);

    f16v acc[2];
#pragma unroll
    for (int i = 0; i < 16; ++i) { acc[0][i] = 0.f; acc[1][i] = 0.f; }
    float cs0 = 0.f, cs1 = 0.f;
    v4u stg;
    u64 tag;
    float A0, A1;
    unsigned tw[4], tr[9], pra;
    {
      v4u fB[2][4], fA[2][2][4];
      auto rdB = [&](int i, int k) { fB[i & 1][k] = *lp<v4u>(aB[slot_of(i) >= 98304][k] + ((slot_of(i) - 32768) & 32768)); };
      auto rdA = [&](int i, int m, int k) { fA[i & 1][m][k] = trfrag(aA[slot_of(i) >= 98304][m][k] + ((slot_of(i) - 32768) & 32768)); };
      auto mm = [&](int i, int m, int k) { acc[m] = mfma32(fA[i & 1][m][k], fB[i & 1][k], acc[m]); };
      auto csum = [&](int i, int j) {
        cs0 += bflo(fA[i & 1][0][0][j]); cs1 += bflo(fA[i & 1][1][0][j]); OPQ(cs0); OPQ(cs1);
        cs0 += bfhi(fA[i & 1][0][0][j]); cs1 += bfhi(fA[i & 1][1][0][j]); OPQ(cs0); OPQ(cs1);
      };
#pragma unroll
      for (int i = 0; i < 7; ++i) {
        if (i == 0) asm volatile("s_waitcnt vmcnt(4) lgkmcnt(0)\n\ts_barrier" : "+v"(Ev), "+v"(pb1a), "+v"(pb1b) :: "memory");
        else if (i == 5) asm volatile("s_waitcnt vmcnt(5)\n\ts_barrier" ::: "memory");
        else asm volatile("s_waitcnt vmcnt(4)\n\ts_barrier" ::: "memory");
        if (i == 0) STAMP(3);
        if (i == 1) STAMP(4);
        if (i == 3) STAMP(19);
        SB();
#pragma unroll
        for (int k = 0; k < 4; ++k) {
          if (i > 0) mm(i - 1, 0, k);
          SB();
          if (i == 4 && k == 0) ALD_B128S(stg, vst, p.ws, sst);
          if (i == 0 && k == 0) {
            kmm(2);
            unsigned pwa = w * 3072 + lane * 16; OPQ(pwa);
            *lp<f4>(pwa) = kc[0]; *lp<f4>(pwa + 1024) = kc[1];
            *lp<f2>(YSC + hg * 8) = f2{ysc, 0.f};
            *lp<f4>(pwa + 2048) = kc[2];
            SB();
          }
          if (i <= 4) { if (k == 0) SETM0(m0w + slot_of(i + 2)); dmaD(i + 2, k); } else dmaW(i - 5, k);
          SB();
          rdA(i, 0, k);
          if (i > 0) csum(i - 1, k);
          SB();
          if (i > 0) mm(i - 1, 1, k);
          SB();
          rdB(i, k); rdA(i, 1, k);
          SB();
        }
        if (i == 0) {
          pra = hg * 12288 + (tg < 3 ? tg : 2) * 1024 + lane * 16; OPQ(pra);
          tag = (((u64)(unsigned)__builtin_amdgcn_readfirstlane(Ev.y) << 32) | (unsigned)__builtin_amdgcn_readfirstlane(Ev.x)) + 1;
          const unsigned t3 = tg < 3 ? tg : 2, tb0 = TTAB + 1536 * hg;
          const unsigned shL = t3 == 0 ? 4u : t3 == 1 ? 3u : 1u, shH = t3 == 0 ? 4u : t3 == 1 ? 2u : 1u;
          const unsigned s1L = t3 < 2 ? 4u : 64u, s1H = t3 == 0 ? 4u : 64u, s0 = t3 == 2 ? 32u : 2u;
          tw[0] = tb0 + 512 * t3 + 256 * hf + (t3 == 0 ? 8u : 128u) * (q4 & 1) + (l15 << (hf ? shH : shL));
          tw[1] = tw[0] + s0; tw[2] = tw[0] + (hf ? s1H : s1L); tw[3] = tw[2] + s0;
          const unsigned g = 255 - (tid & 255);
          const unsigned rL = tg < 2 ? 3u : tg == 2 ? 2u : 1u, rH = tg < 2 ? 3u : tg == 2 ? 2u : 0u, rho = hf ? rH : rL;
          const unsigned oL = tg < 2 ? 0u : tg == 2 ? 0x02000000u : 0x04020000u, oH = tg == 3 ? 0x06040200u : oL;
          const unsigned pL = tg < 2 ? 0x02020200u : tg == 2 ? 0x04040202u : 0x08060404u, pH = tg == 3 ? 0x0e0c0a08u : pL;
          unsigned olo = hf ? oH : oL, ohi = (hf ? pH : pL) | ((2 * g) & (tg < 2 ? 2u : 0u));
          OPQ(olo); OPQ(ohi);
          const unsigned ta = tb0 + (rho << 8) + ((16 * g) >> rho);
#pragma unroll
          for (int e = 0; e < 8; ++e) tr[7 - e] = ta + (((e < 4 ? olo : ohi) >> (8 * (e & 3))) & 0xffu);
          tr[8] = g == 0 ? YSC + 4 : ta - (g <= 32 ? 2u : 0u);
#pragma unroll
          for (int e = 0; e < 4; ++e) OPQ(tw[e]);
#pragma unroll
          for (int e = 0; e < 9; ++e) OPQ(tr[e]);
          SB();
        }
      }
      STAMP(21);
      mm(6, 0, 0); SB(); csum(6, 0); csum(6, 1); SB(); mm(6, 1, 0); SB(); csum(6, 2); csum(6, 3);
      *lp<f2>(CSX + w * 512 + lane * 8) = f2{cs0, cs1};
      SB();
      mm(6, 0, 1); mm(6, 1, 1);
      SB();
      BAR();
      STAMP(5);
      unsigned ca = CSX + hg * 2048 + lane * 8; OPQ(ca);
      const f2 c0 = *lp<f2>(ca), c1 = *lp<f2>(ca + 512), c2 = *lp<f2>(ca + 1024), c3 = *lp<f2>(ca + 1536);
      SB();
      mm(6, 0, 2); mm(6, 0, 3); mm(6, 1, 2); mm(6, 1, 3);
      SB();
      const f2 c = (c0 + c1) + (c2 + c3);
      auto s0 = __builtin_amdgcn_permlane32_swap(asu(c.x), asu(c.x), false, false);
      auto s1 = __builtin_amdgcn_permlane32_swap(asu(c.y), asu(c.y), false, false);
      float t0 = asf(s0[0]) + asf(s0[1]), t1 = asf(s1[0]) + asf(s1[1]);
      asm volatile("" : "+v"(t0), "+v"(t1));
      A0 = hf ? bflo(pb1a) : t0; A1 = hf ? bflo(pb1b) : t1;
    }
    const v2u tg2 = {(unsigned)tag, (unsigned)(tag >> 32)};
    float av;
    {
      asm volatile("s_waitcnt vmcnt(8)" : "+v"(stg) :: "memory");
      if (__builtin_expect(__builtin_amdgcn_ballot_w64(stg.y != tg2.x || stg.w != tg2.x) != 0, 0)) {
#pragma unroll 1
        for (unsigned n = 0; n < SPIN_CAP; ++n) {
          asm volatile("buffer_load_dwordx4 %0, %1, %2, %3 offen sc1\n\ts_waitcnt vmcnt(0)" : "=&v"(stg) : "v"(vst), "s"(p.ws), "s"(sst) : "memory");
          if (__builtin_amdgcn_ballot_w64(stg.y != tg2.x || stg.w != tg2.x) == 0) break;
          __builtin_amdgcn_s_sleep(1);
        }
      }
      const float Bv = asf(hf ? stg.z : stg.x);
      av = __builtin_amdgcn_rcpf(asf(stg.z));
      acc[0] = __builtin_amdgcn_mfma_f32_32x32x2f32(A0, Bv, acc[0], 0, 0, 0);
      acc[1] = __builtin_amdgcn_mfma_f32_32x32x2f32(A1, Bv, acc[1], 0, 0, 0);
    }
    STAMP(6);
    aW[1][0] = aW[0][0] + 65536; aW[1][1] = aW[0][1] + 65536; OPQ(aW[1][0]); OPQ(aW[1][1]);
    unsigned vh = WS_H + ((4 * tt + tg) * 112 + 8 * sl + 4 * hg) * 1024 + lane * 16;
    f2 gc1 = {-0.10294324f, -0.10294324f}, gc2 = {-2.3022082f, -2.3022082f};
    OPQ(vh); OPQ(gc1); OPQ(gc2);
    auto gelu_q = [&](int qq, int wi) {
      v4u o;
#pragma unroll
      for (int j = 0; j < 4; ++j) {
        const int b = 8 * (qq & 1) + 2 * j;
        const f2 v = f2{acc[qq >> 1][b], acc[qq >> 1][b + 1]} * av;
        const f2 z = v * fma2(v * v, gc1, gc2);
        const f2 d = f2{__builtin_amdgcn_exp2f(z.x), __builtin_amdgcn_exp2f(z.y)} + 1.0f;
        unsigned r = pkbf(v * f2{__builtin_amdgcn_rcpf(d.x), __builtin_amdgcn_rcpf(d.y)});
        if (wi >= 0) { OPQ(r); if (j == 0) LGK0(); dmaW(wi, j); }
        o[j] = r;
      }
      st128(o, p.ws, vh + qq * 1024, 0, 16);
    };
    v2u xf;
    v4u uv;
    unsigned vf = (w * 4 + q4) * 128; OPQ(vf);
    ALD_B64S(xf, vf, p.ws, WS_FX);
    {
      const f4 p0 = *lp<f4>(pra), p1 = *lp<f4>(pra + 3072), p2 = *lp<f4>(pra + 6144), p3 = *lp<f4>(pra + 9216);
      *lp<unsigned>(UARR + (w >> 1) * UCP + min((w & 1) * 64 + lane, (w & 2) ? 111u : 112u) * 4) = 0;
      SB();
      gelu_q(0, 2);
      SB();
      const f4 v4 = (p0 + p1) + (p2 + p3);
      const unsigned h0 = pkh(v4.xy), h1 = pkh(v4.zw);
      *lp<u16>(tw[0]) = (u16)h0; *lp<u16>(tw[1]) = (u16)(h0 >> 16); *lp<u16>(tw[2]) = (u16)h1; *lp<u16>(tw[3]) = (u16)(h1 >> 16);
      SB();
    }
    gelu_q(1, -1);
    asm volatile("s_waitcnt vmcnt(6)" : "+v"(xf) :: "memory");
    await64(xf, p.ws, vf, WS_FX, tag);
    ALD_B128S(uv, tid * 16, p.ws, WS_XN + cp * 8192);
    BAR();
    {
      unsigned tv[9];
#pragma unroll
      for (int e = 0; e < 9; ++e) tv[e] = *lp<u16>(tr[e]);
      *lp<unsigned>(q4 * KCP + 4096 + l15 * 4) = 0;
      readW(0);
      SB();
      gelu_q(2, -1);
      SB();
      unsigned r0 = tv[0] | tv[1] << 16, r1 = tv[2] | tv[3] << 16, r2 = tv[4] | tv[5] << 16, r3 = tv[6] | tv[7] << 16;
      OPQ(r0); OPQ(r1); OPQ(r2); OPQ(r3);
      unsigned kb = hg * 2 * KCP + (tid & 255) * 16; OPQ(kb);
      *lp<v4u>(kb + KCP) = v4u{r0, r1, r2, r3};
      *lp<v4u>(kb) = v4u{__builtin_amdgcn_alignbit(r1, r0, 16), __builtin_amdgcn_alignbit(r2, r1, 16), __builtin_amdgcn_alignbit(r3, r2, 16), __builtin_amdgcn_alignbit(tv[8], r3, 16)};
      readW(1);
      SB();
    }
    gelu_q(3, -1);
    STAMP(7);
    {
      asm volatile("s_waitcnt vmcnt(2)" : "+v"(uv) :: "memory");
      const unsigned a0 = __builtin_amdgcn_perm(uv.y, uv.x, 0x05040100), a1 = __builtin_amdgcn_perm(uv.w, uv.z, 0x05040100);
      const unsigned c0 = __builtin_amdgcn_perm(uv.y, uv.x, 0x07060302), c1 = __builtin_amdgcn_perm(uv.w, uv.z, 0x07060302);
      unsigned ub = UARR + 448 + tid * 8, ub2 = ub + 2 * UCP; OPQ(ub); OPQ(ub2);
      *lp<unsigned>(ub + 4) = a0; *lp<unsigned>(ub + 8) = a1;
      *lp<v2u>(ub + UCP) = v2u{a0, a1};
      *lp<unsigned>(ub2 + 4) = c0; *lp<unsigned>(ub2 + 8) = c1;
      *lp<v2u>(ub2 + UCP) = v2u{c0, c1};
      readW(2);
    }
    v4u H[14];
    unsigned sh0 = WS_H + (2 * tb * 112 + 14 * w) * 1024; OPQS(sh0);
    unsigned vl = lane * 16; OPQ(vl);
    auto loadH = [&](int half, int k) { return ld128(p.ws, vl + (k & 3) * 1024, sh0 + half * 112 * 1024 + (k >> 2) * 4096, 16); };
    {
      const unsigned jh = w & 1, thh = (w >> 1) & 1;
      f4 ya[4];
#pragma unroll
      for (int i = 0; i < 4; ++i) ya[i] = f4{0.f, 0.f, 0.f, 0.f};
      unsigned fa = (hg * 2 + (l15 & 1)) * KCP + 2 * (2044 - 1792 - 96 - 128 * jh - 4 * (l15 >> 1) + 8 * q4);
      unsigned ga = UARR + (hg * 2 + (l15 & 1)) * UCP + 2 * (224 - 96 - 128 * jh + 32 * (l15 >> 1) + 8 * q4);
      unsigned fb = fa + 8;
      unsigned vq = hf * 4096 + min(((14 * w) >> 2) + ((lane >> 2) & 7), (14 * w + 13) >> 2) * 128;
      OPQ(fa); OPQ(fb); OPQ(ga); OPQ(vq);
      unsigned dskp;
      v2u hfl;
      STAMP(17);
      BAR();
      STAMP(9);
      auto hook = [&](auto M) {
        constexpr int m = M.v, r = m - HST;
        if constexpr (m < 24 && m % 2 == 0) dmaW(3 + m / 8, (m / 2) & 3);
        if constexpr (m == 5) { if (hg) { VMW(3); if (lane == 0) st64(tg2, p.ws, 0, WS_FH + (4 * tt + tg) * 4096 + (2 * sl + hg) * 128, 16); } }
        if constexpr (m == 15) { if (!hg) { VMW(8); if (lane == 0) st64(tg2, p.ws, 0, WS_FH + (4 * tt + tg) * 4096 + (2 * sl + hg) * 128, 16); } }
        if constexpr (m == 23) { VMW(8); readW(3); }
        if constexpr (m == 27) asm volatile("s_waitcnt lgkmcnt(4)" ::: "memory");
        if constexpr (m >= 27 && m < 35 && m % 2 == 1) dmaW(6, ((m - 27) / 2) & 3);
        if constexpr (m == HST - 8) { dskp = ld32(p.Dsk, 0, cp * 4, 0); hfl = ld64(p.ws, vq, WS_FH + tb * 8192, 16); }
        if constexpr (r == 0) { VMW(0); await64(hfl, p.ws, vq, WS_FH + tb * 8192, tag); STAMP(18); }
        if constexpr (r >= 0 && r % 2 == 0 && r / 2 < 14) H[r / 2] = loadH(0, r / 2);
        if constexpr (r >= 0 && r % 2 == 1 && r / 2 < 12) readW1(4 + r / 8, (r / 2) & 3);
      };
      if (thh) conv_all<1>(fa, fb, ga, ya, hook); else conv_all<0>(fa, fb, ga, ya, hook);
      STAMP(10);
      unsigned y0a = YBUF + w * 4096 + 4 * (32 * (l15 >> 1) + ((2 * (l15 & 1) + 8 * q4) ^ (4 * (l15 >> 1)))), y0b = y0a ^ 16;
      OPQ(y0a); OPQ(y0b);
#pragma unroll
      for (int i = 0; i < 4; ++i) { *lp<f2>(y0a + i * 1024) = ya[i].xy; *lp<f2>(y0b + i * 1024) = ya[i].zw; SB(); }
      unsigned yr = YBUF + (((w + 1) >> 1) & 1) * 8192 + (w >> 1) * 1024 + ((lane ^ (lane >> 3)) << 4); OPQ(yr);
      BAR();
      STAMP(11);
      const f2 sc2 = (*lp<f4>(YSC)).xz;
      const f4 y0 = *lp<f4>(yr) + *lp<f4>(yr + 4096), y1 = *lp<f4>(yr + 16384) + *lp<f4>(yr + 20480);
      const f2 D2 = bf2(dskp);
      v4u sv;
#pragma unroll
      for (int i = 0; i < 4; ++i) sv[i] = pkbf(fma2(f2{y0[i], y1[i]}, sc2, h2f(uv[i]) * D2));
      st128(sv, p.ws, WS_S + cp * 8192 + tid * 16, 0, 16);
      BAR();
    }
    STAMP(12);
    const unsigned etok = tb * 64 + (tid >> 3), ech = cb * 64 + (tid & 7) * 8;
    unsigned pw[8];
    v2u sfl;
    v4u xe, b2e;
    v4u sq;
    auto pubS = [&] { if (lane == 0) st64(tg2, p.ws, 0, WS_FS + ((cp >> 5) * 8 + w) * 4096 + (cp & 31) * 128, 16); };
    if (hg) { VMW(0); pubS(); }
    SB();
#pragma unroll
    for (int half = 0; half < 2; ++half) {
      f16v o0, o1;
#pragma unroll
      for (int i = 0; i < 16; ++i) { o0[i] = 0.f; o1[i] = 0.f; }
#pragma unroll
      for (int k = 0; k < 14; ++k) {
        o0 = mfma32(W[k >> 1][2 * (k & 1)], H[k], o0);
        o1 = mfma32(W[k >> 1][2 * (k & 1) + 1], H[k], o1);
        if (half == 0) H[k] = loadH(1, k);
        SB();
        if (half == 0 && k == 12) {
          if (hg == 0) { VMW(13); pubS(); }
          SB();
        }
        if (half == 0 && k == 10) {
          const unsigned pb = w * 16384 + l31 * 256 + (((hf * 8) ^ (l31 & 7) ^ ((l31 & 2) << 2)) << 4);
#pragma unroll
          for (int g = 0; g < 8; ++g) { pw[g] = pb ^ (g << 4); OPQ(pw[g]); }
          SB();
        }
        if (half == 1 && k == 1) { sfl = ld64(p.ws, vf, WS_FS + (cb * 8 + (tb >> 2)) * 4096, 16); SB(); }
      }
      if (half == 0) {
        xe = ld128(p.x, etok * 896 + ech * 2, 0, 0);
        b2e = ld128(p.b2, ech * 2, 0, 0);
        STAMP(13);
      } else {
        STAMP(14);
        await64(sfl, p.ws, vf, WS_FS + (cb * 8 + (tb >> 2)) * 4096, tag);
        STAMP(22);
        sq = ld128(p.ws, (tid >> 4) * 8192 + l15 * 16, WS_S + cb * 32 * 8192 + tb * 256, 16);
      }
#pragma unroll
      for (int g = 0; g < 4; ++g) {
        *lp<f4>(pw[g] + half * 8192) = f4{o0[4 * g], o0[4 * g + 1], o0[4 * g + 2], o0[4 * g + 3]};
        *lp<f4>(pw[4 + g] + half * 8192) = f4{o1[4 * g], o1[4 * g + 1], o1[4 * g + 2], o1[4 * g + 3]};
      }
      SB();
    }
    *lp<v4u>(SBUF + (tid >> 4) * 256 + ((l15 ^ w) << 4)) = sq;
    STAMP(23);
    {
      const unsigned row = tid >> 3;
      unsigned sr = SBUF + (tid & 7) * 1024 + (((row >> 2) ^ (tid & 7)) << 4) + (row & 3) * 4; OPQ(sr);
      unsigned pa0 = row * 256 + (((tid & 7) ^ (row & 7) ^ ((row & 2) << 2)) << 4), pb0 = pa0 ^ 128, pa1 = pa0 + 65536, pb1 = pb0 + 65536;
      OPQ(pa0); OPQ(pb0); OPQ(pa1); OPQ(pb1);
      BAR();
      STAMP(15);
      f4 a[8], b[8];
#pragma unroll
      for (int k = 0; k < 8; ++k) { a[k] = *lp<f4>((k < 4 ? pa0 : pa1) + (k & 3) * 16384); b[k] = *lp<f4>((k < 4 ? pb0 : pb1) + (k & 3) * 16384); }
      unsigned se[4];
#pragma unroll
      for (int i = 0; i < 4; ++i) se[i] = *lp<unsigned>(sr + i * 256);
      SB();
      const f4 sa = ((a[0] + a[1]) + (a[2] + a[3])) + ((a[4] + a[5]) + (a[6] + a[7]));
      const f4 sb = ((b[0] + b[1]) + (b[2] + b[3])) + ((b[4] + b[5]) + (b[6] + b[7]));
      v4u ov;
#pragma unroll
      for (int i = 0; i < 4; ++i) {
        const f2 o = (i == 0 ? sa.xy : i == 1 ? sa.zw : i == 2 ? sb.xy : sb.zw) + bf2(b2e[i]);
        const f2 z = o * -1.4426950408889634f;
        const f2 d = f2{__builtin_amdgcn_exp2f(z.x), __builtin_amdgcn_exp2f(z.y)} + 1.0f;
        const f2 g = o * f2{__builtin_amdgcn_rcpf(d.x), __builtin_amdgcn_rcpf(d.y)};
        ov[i] = pkbf(fma2(g, bf2(se[i]), bf2(xe[i])));
      }
      st128(ov, p.out, etok * 896 + ech * 2, 0, 16);
    }
    STAMP(16);
  }
  asm volatile("s_waitcnt lgkmcnt(0)" : "+s"(ert0), "+s"(ert) :: "memory");
  if (blockIdx.x == ENCB && w == ENCW) {
    u64 val = ENC == 99 ? 0 : ert - ert0; if (val > 5000) val = 5000;
    u64 target = ert0 + 4000 + 4 * val;
    for (int n = 0; n < 100000; ++n) { if (__builtin_amdgcn_s_memrealtime() >= target) break; __builtin_amdgcn_s_sleep(1); }
  }
}

extern "C" void kernel_launch(void* const* d_in, const int* in_sizes, int n_in, void* d_out, int out_size, void* d_ws, size_t ws_size,
                              hipStream_t stream) {
  const unsigned FL = 0x00020000u;
  HostParams h = {{{d_in[1], DM * 512, FL}, {d_in[2], DM * 512, FL}, {d_in[3], DM * 512, FL}, {d_in[4], DM * 512, FL},
                   {d_ws, WS_END, FL}, {d_in[0], LT * DM * 2, FL}, {d_in[8], DM * FF * 2 + 4096, FL}, {d_in[9], FF * 2, FL},
                   {d_in[5], DM * 2, FL}, {d_in[10], FF * DM * 2, FL}, {d_in[6], DM * 2, FL}, {d_in[11], DM * 2, FL},
                   {d_out, LT * DM * 2, FL}}};
  static_assert(sizeof(HostParams) == sizeof(Params), "descriptor block layout");
  Params p;
  __builtin_memcpy(&p, &h, sizeof(p));
  s4d_mlp_gate_kernel<<<256, 512, 0, stream>>>(p);
}
```

```cpp
#include <hip/hip_runtime.h>
#include <hip/hip_bf16.h>

typedef unsigned short u16;
typedef unsigned long long u64;
typedef int v4i __attribute__((ext_vector_type(4)));
typedef unsigned v4u __attribute__((ext_vector_type(4)));
typedef unsigned v2u __attribute__((ext_vector_type(2)));
typedef float f2 __attribute__((ext_vector_type(2)));
typedef float f4 __attribute__((ext_vector_type(4)));
typedef float f16v __attribute__((ext_vector_type(16)));
typedef short s4 __attribute__((ext_vector_type(4)));
typedef unsigned short us2 __attribute__((ext_vector_type(2)));
typedef __bf16 b8 __attribute__((ext_vector_type(8)));
typedef __bf16 b2 __attribute__((ext_vector_type(2)));
typedef _Float16 h8 __attribute__((ext_vector_type(8)));
typedef _Float16 h2 __attribute__((ext_vector_type(2)));
#define LAS __attribute__((address_space(3)))
#define DEV __device__ __forceinline__
#define SB() __builtin_amdgcn_sched_barrier(0)
#define VMW(n) asm volatile("s_waitcnt vmcnt(" #n ")" ::: "memory")
#define LGK0() asm volatile("s_waitcnt lgkmcnt(0)" ::: "memory")
#define BAR() asm volatile("s_waitcnt lgkmcnt(0)\n\ts_barrier" ::: "memory")
#define OPQ(x) asm volatile("" : "+v"(x))
#define OPQS(x) asm volatile("" : "+s"(x))

#define STAMPS 0
#define STAMPI "s_memtime"
#define STAMP(i) do {} while (0)

constexpr int LT = 2048, DM = 448, FF = 1792;
constexpr unsigned XNS = 224 * 256 + 256, SST = 16384 + 256;
constexpr unsigned WS_E = 0, WS_FX = 4096, WS_CS = 16384, WS_ST = 32768, WS_FH = 65536, WS_XN = WS_FH + 64 * 4096,
                   WS_S = WS_XN + 32 * XNS, WS_H = WS_S + 224 * SST, WS_END = WS_H + 64 * 112 * 1024, WS_DBG = 24u << 20;
constexpr unsigned KCP = 4224, KROW = 288, KSCR = 16 * KROW, TTAB = 28672, UARR = 65536, UCP = 4640, YSC = 31744, BSYN = 31760, BSY2 = 84224, YBUF = 131072, SBUF = 131072;
constexpr unsigned wstg_of(int i) { return 32768u + 65536u * ((i >> 1) & 1) + 16384u * (i & 1); }
constexpr unsigned slot_of(int i) { return 32768u + ((i + 2) & 3) * 32768u; }
#define XB 1
#define NHA 18
#define NWG 6
#define NTB 0
#define NTC 3
#define BPRIO 1
#define NRAW 28
#define NHB 18
#define GST 1
#define GSB() SB()
#define NAP0 4
#define SK 8
#define NHE 0
#define CSI 5
#define MTCH 0
#define TS0 1
#define R5 2
#define UPS 5
#define XG1 3
#define XG2 5
constexpr unsigned CODE_PF_BYTES = 27648;
constexpr unsigned SPIN_CAP = 1u << 14;
constexpr unsigned OOB = 0x70000000u;

struct Buf { const void* p; unsigned bytes, flags; };
struct HostParams { Buf b[13]; };
struct Params { v4i lA, Ai, Cr, Ci, ws, x, w1, b1, ldt, w2, Dsk, b2, out; };

__device__ v4u ld128(v4i rs, unsigned vo, unsigned so, int aux) __asm("llvm.amdgcn.raw.buffer.load.v4i32");
__device__ v2u ld64(v4i rs, unsigned vo, unsigned so, int aux) __asm("llvm.amdgcn.raw.buffer.load.v2i32");
__device__ unsigned ld32(v4i rs, unsigned vo, unsigned so, int aux) __asm("llvm.amdgcn.raw.buffer.load.i32");
__device__ void st128(v4u v, v4i rs, unsigned vo, unsigned so, int aux) __asm("llvm.amdgcn.raw.buffer.store.v4i32");
__device__ void st64(v2u v, v4i rs, unsigned vo, unsigned so, int aux) __asm("llvm.amdgcn.raw.buffer.store.v2i32");

DEV float asf(unsigned u) { return __builtin_bit_cast(float, u); }
DEV unsigned asu(float f) { return __builtin_bit_cast(unsigned, f); }
DEV float bflo(unsigned u) { return asf(u << 16); }
DEV float bfhi(unsigned u) { return asf(u & 0xffff0000u); }
DEV f2 bf2(unsigned u) { return f2{bflo(u), bfhi(u)}; }
DEV unsigned pkbf(f2 v) { return __builtin_bit_cast(unsigned, __builtin_convertvector(v, b2)); }
DEV unsigned pkh(f2 v) { return __builtin_bit_cast(unsigned, __builtin_convertvector(v, h2)); }
DEV float rbf(float a) { return bflo(pkbf(f2{a, a})); }
DEV f2 h2f(unsigned u) { h2 h = __builtin_bit_cast(h2, u); return f2{(float)h.x, (float)h.y}; }
DEV f2 fma2(f2 a, f2 b, f2 c) { return __builtin_elementwise_fma(a, b, c); }
DEV float dot2(unsigned a, unsigned b, float s) { return __builtin_amdgcn_fdot2_f32_bf16(__builtin_bit_cast(b2, a), __builtin_bit_cast(b2, b), s, false); }
template <typename T> DEV LAS T* lp(unsigned a) { return (LAS T*)(size_t)a; }
#define ALD_U16(d, vo, rs, so) asm volatile("buffer_load_ushort %0, %1, %2, %3 offen" : "=v"(d) : "v"(vo), "s"(rs), "s"(so) : "memory")
#define ALD_B32(d, vo, rs, so) asm volatile("buffer_load_dword %0, %1, %2, %3 offen" : "=v"(d) : "v"(vo), "s"(rs), "s"(so) : "memory")
#define ALD_B64(d, vo, rs, so) asm volatile("buffer_load_dwordx2 %0, %1, %2, %3 offen" : "=v"(d) : "v"(vo), "s"(rs), "s"(so) : "memory")
#define ALD_B64S(d, vo, rs, so) asm volatile("buffer_load_dwordx2 %0, %1, %2, %3 offen sc1" : "=v"(d) : "v"(vo), "s"(rs), "s"(so) : "memory")
#define ALD_B128S(d, vo, rs, so) asm volatile("buffer_load_dwordx4 %0, %1, %2, %3 offen sc1" : "=v"(d) : "v"(vo), "s"(rs), "s"(so) : "memory")
DEV v4u trfrag(unsigned a) {
  s4 lo = __builtin_amdgcn_ds_read_tr16_b64_v4i16(lp<s4>(a)), hi = __builtin_amdgcn_ds_read_tr16_b64_v4i16(lp<s4>(a + 1024));
  v2u l = __builtin_bit_cast(v2u, lo), h = __builtin_bit_cast(v2u, hi);
  return v4u{l.x, l.y, h.x, h.y};
}
DEV f16v mfma32(v4u a, v4u b, f16v c) { return __builtin_amdgcn_mfma_f32_32x32x16_bf16(__builtin_bit_cast(b8, a), __builtin_bit_cast(b8, b), c, 0, 0, 0); }
DEV f4 mfma16h(v4u a, v4u b, f4 c) { return __builtin_amdgcn_mfma_f32_16x16x32_f16(__builtin_bit_cast(h8, a), __builtin_bit_cast(h8, b), c, 0, 0, 0); }
template <int C> DEV float dppf(float v) { return asf(__builtin_amdgcn_update_dpp(0, asu(v), C, 0xf, 0xf, true)); }
DEV float sum8(float v) { v += dppf<0xB1>(v); v += dppf<0x4E>(v); v += dppf<0x141>(v); return v; }
template <int C, int RM> DEV unsigned dppu(unsigned v) { return __builtin_amdgcn_update_dpp(0, v, C, RM, 0xf, true); }
DEV unsigned wave_maxu(unsigned v) {
  v = max(v, dppu<0xB1, 0xf>(v)); v = max(v, dppu<0x4E, 0xf>(v)); v = max(v, dppu<0x141, 0xf>(v)); v = max(v, dppu<0x140, 0xf>(v));
  v = max(v, dppu<0x142, 0xa>(v)); v = max(v, dppu<0x143, 0xc>(v));
  return __builtin_amdgcn_readlane(v, 63);
}
DEV u64 as64(v2u v) { return __builtin_bit_cast(u64, v); }
template <int NAP = NAP0> DEV void await64(v2u f, v4i rs, unsigned vo, unsigned so, u64 tag) {
  if (__builtin_expect(__builtin_amdgcn_ballot_w64(as64(f) != tag) != 0, 0)) {
#pragma unroll 1
    for (unsigned n = 0; n < SPIN_CAP; ++n) {
      asm volatile("buffer_load_dwordx2 %0, %1, %2, %3 offen sc1\n\ts_waitcnt vmcnt(0)" : "=&v"(f) : "v"(vo), "s"(rs), "s"(so) : "memory");
      if (__builtin_amdgcn_ballot_w64(as64(f) != tag) == 0) break;
      __builtin_amdgcn_s_sleep(NAP);
    }
  }
}
struct cm { f2 w, r; };
DEV cm mkcm(f2 w) { return cm{w, f2{-w.y, w.x}}; }
DEV f2 cmul(f2 a, cm m) { return fma2(a.xx, m.w, a.yy * m.r); }
DEV void kputA(const unsigned (&v)[16]) {
  asm volatile("ds_write_addtid_b32 %0 offset:0\n\tds_write_addtid_b32 %1 offset:288\n\tds_write_addtid_b32 %2 offset:576\n\tds_write_addtid_b32 %3 offset:864\n\t"
               "ds_write_addtid_b32 %4 offset:1152\n\tds_write_addtid_b32 %5 offset:1440\n\tds_write_addtid_b32 %6 offset:1728\n\tds_write_addtid_b32 %7 offset:2016\n\t"
               "ds_write_addtid_b32 %8 offset:2304"
               :: "v"(v[0]), "v"(v[1]), "v"(v[2]), "v"(v[3]), "v"(v[4]), "v"(v[5]), "v"(v[6]), "v"(v[7]), "v"(v[8]) : "memory");
}
DEV void kputB(const unsigned (&v)[16]) {
  asm volatile("ds_write_addtid_b32 %0 offset:2592\n\tds_write_addtid_b32 %1 offset:2880\n\tds_write_addtid_b32 %2 offset:3168\n\tds_write_addtid_b32 %3 offset:3456\n\t"
               "ds_write_addtid_b32 %4 offset:3744\n\tds_write_addtid_b32 %5 offset:4032\n\tds_write_addtid_b32 %6 offset:4320"
               :: "v"(v[9]), "v"(v[10]), "v"(v[11]), "v"(v[12]), "v"(v[13]), "v"(v[14]), "v"(v[15]) : "memory");
}
static_assert(KROW == 288, "offsets above");
#define SETM0(x) asm volatile("s_mov_b32 m0, %0\n\ts_nop 0" :: "s"(x) : "memory")
#define DMA(vo, rs, so, imm) asm volatile("buffer_load_dwordx4 %0, %1, %2 offen offset:" #imm " lds" :: "v"(vo), "s"(rs), "s"(so) : "memory")

struct z2 { float r0, i0, r1, i1; };
DEV void zmul(z2& z, const z2& w) {
  float t0, t1, t2, t3;
  asm("v_mul_f32 %4, %1, %9\n\tv_mul_f32 %5, %1, %8\n\tv_mul_f32 %6, %3, %11\n\tv_mul_f32 %7, %3, %10\n\t"
      "v_fma_f32 %1, %0, %9, %5\n\tv_fma_f32 %0, %0, %8, -%4\n\tv_fma_f32 %3, %2, %11, %7\n\tv_fma_f32 %2, %2, %10, -%6"
      : "+v"(z.r0), "+v"(z.i0), "+v"(z.r1), "+v"(z.i1), "=&v"(t0), "=&v"(t1), "=&v"(t2), "=&v"(t3) : "v"(w.r0), "v"(w.i0), "v"(w.r1), "v"(w.i1));
}
DEV void zstep(z2& z, const z2& w, unsigned& a, unsigned& b) {
  float t0, t1, t2, t3;
  asm volatile("v_mul_f32 %4, %1, %11\n\tv_mul_f32 %5, %1, %10\n\tv_mul_f32 %6, %3, %13\n\tv_mul_f32 %7, %3, %12\n\t"
      "v_cvt_pk_f16_f32 %8, %0, %1\n\tv_cvt_pk_f16_f32 %9, %2, %3\n\t"
      "v_fma_f32 %1, %0, %11, %5\n\tv_fma_f32 %0, %0, %10, -%4\n\tv_fma_f32 %3, %2, %13, %7\n\tv_fma_f32 %2, %2, %12, -%6"
      : "+v"(z.r0), "+v"(z.i0), "+v"(z.r1), "+v"(z.i1), "=&v"(t0), "=&v"(t1), "=&v"(t2), "=&v"(t3), "=&v"(a), "=&v"(b)
      : "v"(w.r0), "v"(w.i0), "v"(w.r1), "v"(w.i1));
}
DEV void zleaf(const z2& z, const z2& w, unsigned& a, unsigned& b) {
  float t0, t1, t2, t3;
  asm volatile("v_mul_f32 %0, %7, %11\n\tv_mul_f32 %1, %7, %10\n\tv_mul_f32 %2, %9, %13\n\tv_mul_f32 %3, %9, %12\n\t"
      "v_fma_f32 %0, %6, %10, -%0\n\tv_fma_f32 %1, %6, %11, %1\n\tv_fma_f32 %2, %8, %12, -%2\n\tv_fma_f32 %3, %8, %13, %3\n\t"
      "v_cvt_pk_f16_f32 %4, %0, %1\n\tv_cvt_pk_f16_f32 %5, %2, %3"
      : "=&v"(t0), "=&v"(t1), "=&v"(t2), "=&v"(t3), "=&v"(a), "=&v"(b)
      : "v"(z.r0), "v"(z.i0), "v"(z.r1), "v"(z.i1), "v"(w.r0), "v"(w.i0), "v"(w.r1), "v"(w.i1));
}
DEV void zpack(const z2& z, unsigned& a, unsigned& b) {
  asm volatile("v_cvt_pk_f16_f32 %0, %2, %3\n\tv_cvt_pk_f16_f32 %1, %4, %5" : "=&v"(a), "=&v"(b) : "v"(z.r0), "v"(z.i0), "v"(z.r1), "v"(z.i1));
}
template <int V> struct IC { static constexpr int v = V; };
template <int B, int E, typename FN> DEV void sfor(FN&& f) { if constexpr (B < E) { f(IC<B>{}); sfor<B + 1, E>(f); } }
constexpr int cv_i(int pm) { int i = 0; while (pm >= 8 - i) { pm -= 8 - i; ++i; } return i; }
constexpr int cv_e(int pm) { int i = 0; while (pm >= 8 - i) { pm -= 8 - i; ++i; } return 7 - i - pm; }
constexpr int cv_first(int i) { return i * 8 - i * (i - 1) / 2; }
template <typename FN> DEV void conv_all(unsigned fa, unsigned fb, unsigned ga, f4 (&acc)[8], FN&& hook) {
  v4u Fv[2][8], Gv[2][8];
  auto ldF = [&](auto J, auto I) {
    v2u a = *lp<v2u>(fa + 512 * (7 - I.v) + 64 * (3 - J.v)), b = *lp<v2u>(fb + 512 * (7 - I.v) + 64 * (3 - J.v));
    Fv[J.v & 1][I.v] = v4u{a.x, a.y, b.x, b.y};
  };
  auto ldG = [&](auto J, auto E) { Gv[J.v & 1][E.v] = *lp<v4u>(ga + 512 * E.v + 64 * (3 - J.v)); };
  ldF(IC<0>{}, IC<0>{}); SB();
  sfor<0, 8>([&](auto E) { ldG(IC<0>{}, IC<7 - E.v>{}); SB(); });
  sfor<0, 144>([&](auto M) {
    constexpr int jj = M.v / 36, pm = M.v % 36, i = cv_i(pm), e = cv_e(pm);
    acc[i + e] = mfma16h(Fv[jj & 1][i], Gv[jj & 1][e], acc[i + e]);
    SB();
    constexpr int n = M.v + 8, nj = n / 36, np = n % 36;
    if constexpr (n < 144 && np == cv_first(cv_i(np))) ldF(IC<nj>{}, IC<cv_i(np)>{});
    if constexpr (jj < 3 && pm >= 11 && pm < 35 && (pm - 11) % 3 == 0) ldG(IC<jj + 1>{}, IC<7 - (pm - 11) / 3>{});
    hook(M);
    SB();
  });
}

__global__ __launch_bounds__(512, 2) void s4d_mlp_gate_kernel(Params p) {
  __shared__ __attribute__((aligned(16))) char smem[163840];
  asm volatile("" :: "v"((LAS char*)smem) : "memory");
  const unsigned tid = threadIdx.x, lane = tid & 63;
  const unsigned w = __builtin_amdgcn_readfirstlane(tid >> 6);
  const unsigned xcd = blockIdx.x & 7, idx = blockIdx.x >> 3;
  {
    u64 pc; asm volatile("s_getpc_b64 %0" : "=s"(pc));
    const v4i rC = {(int)(unsigned)pc, (int)(unsigned)(pc >> 32), (int)CODE_PF_BYTES, 0x00020000};
    asm volatile("s_mov_b32 m0, %0\n\ts_nop 0\n\tbuffer_load_dword %1, %2, %3 offen lds" :: "s"(slot_of(3) + w * 4096),
                 "v"(lane < 4 ? lane * 128 : OOB), "s"(rC), "s"(((blockIdx.x & 56) + w) * 512) : "memory");
    SB();
  }
  if (idx < 4) {
    const unsigned hid = xcd * 4 + idx, tok0 = hid * 64 + w * 8, r8 = lane >> 3, part = lane & 7;
    asm volatile("" :: "s"(p.lA), "s"(p.Ai), "s"(p.Cr), "s"(p.Ci));
    const unsigned cj = (xcd >> 1) * 4 + idx, cbk = 14 * (xcd & 1) + min(cj, 13u);
    unsigned vo = r8 * 896 + part * 16, so = tok0 * 896, vo1 = r8 * 3584 + part * 16, so1 = w * 56 * 3584 + cbk * 128;
    OPQ(vo); OPQS(so); OPQ(vo1); OPQS(so1); SB();
    v4u xr[7], wr[7];
#pragma unroll
    for (int k = 0; k < 7; ++k) xr[k] = ld128(p.x, vo + k * 128, so, 0);
    const v2u Ev = ld64(p.ws, 0, 0, 16);
    SB();
    float s0 = 0.f, s1 = 0.f, q0 = 0.f, q1 = 0.f;
#pragma unroll
    for (int k = 0; k < 7; ++k) {
      s0 = dot2(xr[k].x, 0x3f803f80u, s0); q0 = dot2(xr[k].x, xr[k].x, q0); s1 = dot2(xr[k].y, 0x3f803f80u, s1); q1 = dot2(xr[k].y, xr[k].y, q1);
      s0 = dot2(xr[k].z, 0x3f803f80u, s0); q0 = dot2(xr[k].z, xr[k].z, q0); s1 = dot2(xr[k].w, 0x3f803f80u, s1); q1 = dot2(xr[k].w, xr[k].w, q1);
    }
    STAMP(1);
    const float mean = sum8(s0 + s1) * (1.0f / DM);
    const float var = fmaxf(__builtin_fmaf(-mean, mean, sum8(q0 + q1) * (1.0f / DM)), 0.f);
    const float mb = rbf(mean);
    const float sd1 = rbf(__builtin_amdgcn_sqrtf(rbf(rbf(var) + 1e-5f)));
    const float rs1 = __builtin_amdgcn_rcpf(sd1);
    const float mb2 = rbf((mean - mb) * rs1);
    const float sd2 = rbf(__builtin_amdgcn_sqrtf(rbf(rbf(var * rs1 * rs1) + 1e-5f)));
    const u64 tag = (((u64)(unsigned)__builtin_amdgcn_readfirstlane(Ev.y) << 32) | (unsigned)__builtin_amdgcn_readfirstlane(Ev.x)) + 1;
    const v2u tg2 = {(unsigned)tag, (unsigned)(tag >> 32)};
    st128(v4u{asu(-__builtin_fmaf(mb2, sd1, mb)), tg2.x, asu(sd1 * sd2), tg2.x}, p.ws, part == 0 ? WS_ST + (tok0 + r8) * 16 : OOB, 0, 16);
    STAMP(2);
    SB();
#pragma unroll
    for (int k = 0; k < 7; ++k) wr[k] = ld128(p.w1, vo1, so1 + k * 8 * 3584, 0);
    SB();
    {
      unsigned la = part * 1024 + ((((2 * w + (r8 >> 2)) ^ part) << 4) | ((r8 & 3) << 2));
      OPQ(la);
      const f2 rs2 = {rs1, rs1}, off2 = {-mb * rs1, -mb * rs1};
#pragma unroll
      for (int k = 0; k < 7; ++k)
#pragma unroll
        for (int i = 0; i < 4; ++i) *lp<unsigned>(la + k * 8192 + i * 256) = pkh(fma2(bf2(xr[k][i]), rs2, off2));
    }
    SB();
    {
      f2 c[4];
#pragma unroll
      for (int i = 0; i < 4; ++i) {
        c[i] = ((bf2(wr[0][i]) + bf2(wr[1][i])) + (bf2(wr[2][i]) + bf2(wr[3][i]))) + ((bf2(wr[4][i]) + bf2(wr[5][i])) + bf2(wr[6][i]));
      }
      auto f32 = [](float a, float b) { auto t = __builtin_amdgcn_permlane32_swap(asu(a), asu(b), false, false); return asf(t[0]) + asf(t[1]); };
      auto f16 = [](float a, float b) { auto t = __builtin_amdgcn_permlane16_swap(asu(a), asu(b), false, false); return asf(t[0]) + asf(t[1]); };
      float a = f16(f32(c[0].x, c[2].x), f32(c[1].x, c[3].x)), b = f16(f32(c[0].y, c[2].y), f32(c[1].y, c[3].y));
      a += dppf<0x128>(a); b += dppf<0x128>(b);
      *lp<f2>(61440 + w * 256 + part * 32 + (lane >> 4) * 8) = f2{a, b};
    }
    BAR();
    STAMP(3);
    if (w == 0) {
      float c[8];
#pragma unroll
      for (int k = 0; k < 8; ++k) c[k] = *lp<float>(61440 + k * 256 + lane * 4);
      st64(v2u{asu(((c[0] + c[1]) + (c[2] + c[3])) + ((c[4] + c[5]) + (c[6] + c[7]))), tg2.x}, p.ws, cj < 14 ? lane * 8 : OOB, WS_CS + cbk * 512, 16);
    }
    {
      v4u v[7];
#pragma unroll
      for (int k = 0; k < 7; ++k) v[k] = *lp<v4u>(tid * 16 + k * 8192);
      const unsigned go = WS_XN + hid * XNS + (tid >> 4) * 256 + (((tid & 15) ^ (w & 7)) << 4);
#pragma unroll
      for (int k = 0; k < 7; ++k) st128(v[k], p.ws, go + k * 8192, 0, 16);
    }
    unsigned vt = lane < 56 ? lane * 128 : OOB, st0 = (((w & 3) * 7 + (w >> 2)) * 32 + idx * 8) * 896;
    OPQ(vt); OPQS(st0);
    SETM0(65536 + w * 256);
    auto touch = [&](v4i rs, unsigned vo, unsigned so) { asm volatile("buffer_load_dword %0, %1, %2 offen lds" :: "v"(vo), "s"(rs), "s"(so) : "memory"); };
    VMW(0);
    BAR();
    STAMP(4);
    if (tid == 0) st64(tg2, p.ws, 0, WS_FX + hid * 128, 16);
    if (!MTCH) { touch(p.w2, vt, st0); touch(p.w2, vt, st0 + 2 * 32 * 896); touch(p.w2, vt, st0 + 4 * 32 * 896);
    if (w < 4) touch(p.w2, vt, st0 + 6 * 32 * 896); }
    STAMP(6);
    STAMP(7);
    if (hid == 0) {
      const unsigned vo2 = tid < 224 ? WS_FH + ((tid / 14) * 4 * 4096) + (tid % 14) * 256 : WS_FX + (tid & 31) * 128;
      if (w < 4) await64<24>(v2u{tg2.x + 1, tg2.y}, p.ws, vo2, 0u, tag);
      BAR();
      if (tid == 0) st64(tg2, p.ws, 0, WS_E, 16);
    }
    STAMP(5);
  } else {
    const unsigned ma = idx & 3, mb_ = (idx >> 2) - 1;
    const unsigned tt = 4 * (xcd >> 1) + ma, sl = 7 * (xcd & 1) + mb_, tb = 4 * xcd + ma, cb = mb_, cp = 28 * xcd + idx - 4;
    const bool rot = (ma + mb_) & 1;
    const unsigned wq = w & 3, hf = lane >> 5, l31 = lane & 31, l15 = lane & 15, q4 = lane >> 4;
    unsigned vx0, vx1, vw0, vw1, sxA, swA, sxB, swB, m0d;
    v4i rW1 = p.w1;
    auto dma_setup = [&] {
      vx0 = (lane >> 3) * 896 + (((lane & 7) ^ q4) << 4); vx1 = (vx0 ^ 64) + 8 * 896 - 1024;
      vw0 = q4 * 3584 + ((l15 ^ (q4 << 2)) << 4) + 2048; vw1 = vw0 + 4 * 3584 - 1024;
      sxA = (tt * 128 + wq * 32) * 896 + (rot ? 4 * 128 : 0); swA = wq * 16 * 3584 + sl * 256 + (rot ? 4 * 64 * 3584 : 0);
      m0d = wq * 8192;
      OPQ(vx0); OPQ(vx1); OPQ(vw0); OPQ(vw1); OPQS(sxA); OPQS(swA); OPQS(m0d);
    };
    auto w1_base = [&] { const u64 b = (((u64)(unsigned)rW1.y << 32) | (unsigned)rW1.x) - 4096; rW1.x = (int)(unsigned)b; rW1.y = (int)(unsigned)(b >> 32); };
    auto dmaD = [&](int i, int j, bool setm0) {
      const unsigned sx = (i < 3 ? sxA : sxB) + i * 128 + (j >> 2) * 16 * 896, sw = (i < 3 ? swA : swB) + i * (64 * 3584) + (j >> 2) * 8 * 3584;
      if (setm0 || (j & 3) == 0) SETM0(m0d + slot_of(i) + (j >> 2) * 4096);
      if ((j & 3) == 0) DMA(vx0, p.x, sx, 0); else if ((j & 3) == 1) DMA(vx1, p.x, sx, 1024);
      else if ((j & 3) == 2) DMA(vw0, rW1, sw, 2048); else DMA(vw1, rW1, sw, 3072);
    };
    unsigned vw2, sv0, aW[2][2];
    auto w2_setup = [&](unsigned lo) {
      vw2 = (lane >> 3) * 896 + (((lane & 7) ^ ((q4 & 1) << 2)) << 4); sv0 = w * 7 * 32 * 896 + cb * 128;
      aW[0][0] = lo + wq * 4096 + (4 * hf + (l15 >> 2)) * 128 + (((2 * (q4 & 1) + ((lane & 3) >> 1)) ^ (((l15 >> 3) & 1) << 2)) << 4) + 8 * (lane & 1);
      aW[0][1] = aW[0][0] ^ 64; aW[1][0] = aW[0][0] + 65536; aW[1][1] = aW[0][1] + 65536;
      OPQ(vw2); OPQS(sv0); OPQ(aW[0][0]); OPQ(aW[0][1]); OPQ(aW[1][0]); OPQ(aW[1][1]);
    };
    auto dmaW = [&](int i, int j, unsigned stg) {
      const unsigned s = sv0 + i * (32 * 896);
      if (j == 0) { SETM0(stg + wq * 4096); DMA(vw2, p.w2, s, 0); } else if (j == 1) DMA(vw2, p.w2, s + 6144, 1024);
      else if (j == 2) DMA(vw2, p.w2, s + 12288, 2048); else DMA(vw2, p.w2, s + 18432, 3072);
    };
    auto readW = [&](v4u (&W)[7][4], int i, unsigned off) {
#pragma unroll
      for (int f = 0; f < 4; ++f) W[i][f] = trfrag(aW[off >> 16][f & 1] + (off & 65535) + (f >> 1) * 2048);
    };
    unsigned zero = 0; OPQ(zero);

    auto bwait = [&](unsigned a, unsigned n) {
      while ((unsigned)__builtin_amdgcn_readfirstlane(__hip_atomic_load(lp<unsigned>(a), __ATOMIC_RELAXED, __HIP_MEMORY_SCOPE_WORKGROUP)) < n) {}
      asm volatile("" ::: "memory");
    };
    auto tail = [&](auto ROLE, v4u (&W)[7][4], v4u (&H)[28], auto LA, auto&& loadH, u64 tag) {
      constexpr int role = ROLE.v, la = LA.v;
      unsigned tid = threadIdx.x; OPQ(tid);
      const unsigned lane = tid & 63, hf = lane >> 5, l31 = lane & 31, l15 = lane & 15, q4 = lane >> 4;
      unsigned pw[8];
      f16v o0, o1;
      auto zero_o = [&] {
#pragma unroll
        for (int i = 0; i < 16; ++i) { o0[i] = 0.f; o1[i] = 0.f; }
      };
      auto put_o = [&](const f16v& o, int mc) {
#pragma unroll
        for (int g = 0; g < 4; ++g) *lp<f4>(pw[4 * mc + g]) = f4{o[4 * g], o[4 * g + 1], o[4 * g + 2], o[4 * g + 3]};
      };
      auto set_pw = [&](unsigned base) {
        const unsigned pb = base + (((hf * 8) ^ (l31 & 7) ^ ((l31 & 2) << 2)) << 4);
#pragma unroll
        for (int g = 0; g < 8; ++g) { pw[g] = pb ^ (g << 4); OPQ(pw[g]); }
      };
      auto gate = [&](f2 o, unsigned sv, f2 xv) {
        const f2 z = o * -1.4426950408889634f;
        const f2 d = f2{__builtin_amdgcn_exp2f(z.x), __builtin_amdgcn_exp2f(z.y)} + 1.0f;
        return pkbf(fma2(o * f2{__builtin_amdgcn_rcpf(d.x), __builtin_amdgcn_rcpf(d.y)}, bf2(sv), xv));
      };
      const unsigned so_x = tb * 64 * 896 + cb * 128;
      unsigned ve1 = (32 + (tid >> 4)) * 896 + l15 * 8; OPQ(ve1);
      v2u xe1, be1;
      v4u xe0, be0;
      unsigned ve0 = (tid >> 3) * 896 + (tid & 7) * 16;
      zero_o();
      sfor<0, 14>([&](auto K) {
        constexpr int k = K.v;
        o0 = mfma32(W[k >> 1][2 * (k & 1)], H[k], o0);
        o1 = mfma32(W[k >> 1][2 * (k & 1) + 1], H[k], o1);
        if constexpr (k + la < 28) loadH(k + la);
        if constexpr (k + la == 28) { xe1 = ld64(p.x, ve1, so_x, 0); be1 = ld64(p.b2, l15 * 8, cb * 128, 0); }
        if constexpr (role == 0 && k + la == 29) { OPQ(ve0); xe0 = ld128(p.x, ve0, so_x, 0); be0 = ld128(p.b2, (tid & 7) * 16, cb * 128, 0); }
        SB();
        if constexpr (k == 3) { set_pw((role ? 0 : 32768) + wq * 4096 + (l31 >> 4) * 16384 + l15 * 256); SB(); }
      });
      STAMP(13);
      put_o(o0, 0); put_o(o1, 1);
      zero_o();
      SB();
      f2 xl, xh, bl, bh;
      if constexpr (role == 0) {
        v4u g0, g1, g2, g3;
        unsigned vs = (w * 8 + q4) * SST + l15 * 16, ss = WS_S + cb * 32 * SST + (tb >> 2) * 2048 + (tb & 3) * 256;
        sfor<0, 14>([&](auto K) {
          constexpr int k = K.v;
          o0 = mfma32(W[k >> 1][2 * (k & 1)], H[14 + k], o0);
          o1 = mfma32(W[k >> 1][2 * (k & 1) + 1], H[14 + k], o1);
          if constexpr (k == SK) { OPQ(vs); OPQS(ss); g0 = ld128(p.ws, vs, ss, 16); g1 = ld128(p.ws, vs + 1024, ss, 16); g2 = ld128(p.ws, vs, ss + 4 * SST, 16); g3 = ld128(p.ws, vs + 1024, ss + 4 * SST, 16); }
          SB();
        });
        STAMP(14);
        {
          const unsigned t32 = (unsigned)tag;
          auto stale = [&] { return __builtin_amdgcn_ballot_w64(((g0.y ^ t32) | (g0.w ^ t32) | (g1.y ^ t32) | (g1.w ^ t32) | (g2.y ^ t32) | (g2.w ^ t32) | (g3.y ^ t32) | (g3.w ^ t32)) != 0) != 0; };
          if (__builtin_expect(stale(), 0)) {
#pragma unroll 1
            for (unsigned n = 0; n < SPIN_CAP; ++n) {
              asm volatile("buffer_load_dwordx4 %0, %4, %5, %6 offen sc1\n\tbuffer_load_dwordx4 %1, %4, %5, %6 offen offset:1024 sc1\n\t"
                           "buffer_load_dwordx4 %2, %4, %5, %7 offen sc1\n\tbuffer_load_dwordx4 %3, %4, %5, %7 offen offset:1024 sc1\n\ts_waitcnt vmcnt(0)"
                           : "=&v"(g0), "=&v"(g1), "=&v"(g2), "=&v"(g3) : "v"(vs), "s"(p.ws), "s"(ss), "s"(ss + 4 * SST) : "memory");
              if (!stale()) break;
              __builtin_amdgcn_s_sleep(NAP0);
            }
          }
        }
        const v4u sq0 = {g0.x, g0.z, g1.x, g1.z}, sq1 = {g2.x, g2.z, g3.x, g3.z};
        set_pw(65536 + w * 8192 + l31 * 256);
        const unsigned row = tid >> 3;
        unsigned pa0 = (row >> 4) * 16384 + (row & 15) * 256 + (((tid & 7) ^ (row & 7) ^ ((row & 2) << 2)) << 4), pb0 = pa0 ^ 128;
        unsigned sr = SBUF + (tid & 7) * 1024 + (((row >> 2) ^ (tid & 7)) << 4) + (row & 3) * 4;
        OPQ(pa0); OPQ(pb0); OPQ(sr);
        xl = bf2(xe1.x); xh = bf2(xe1.y); bl = bf2(be1.x); bh = bf2(be1.y);
        OPQ(xl); OPQ(xh); OPQ(bl); OPQ(bh);
        bwait(BSY2, 4);
        *lp<v4u>(SBUF + (w * 8 + q4) * 256 + ((l15 ^ (2 * w)) << 4)) = sq0;
        *lp<v4u>(SBUF + (w * 8 + 4 + q4) * 256 + ((l15 ^ (2 * w + 1)) << 4)) = sq1;
        STAMP(22);
        BAR();
        STAMP(23);
        f4 a[8], b[8];
#pragma unroll
        for (int k = 0; k < 8; ++k) { a[k] = *lp<f4>(pa0 + (k >> 2) * 32768 + (k & 3) * 4096); b[k] = *lp<f4>(pb0 + (k >> 2) * 32768 + (k & 3) * 4096); }
        unsigned se[4];
#pragma unroll
        for (int i = 0; i < 4; ++i) se[i] = *lp<unsigned>(sr + i * 256);
        put_o(o0, 0); put_o(o1, 1);
        SB();
        const f4 sa = ((a[0] + a[1]) + (a[2] + a[3])) + ((a[4] + a[5]) + (a[6] + a[7]));
        const f4 sb = ((b[0] + b[1]) + (b[2] + b[3])) + ((b[4] + b[5]) + (b[6] + b[7]));
        const v4u ov = {gate(sa.xy + bf2(be0.x), se[0], bf2(xe0.x)), gate(sa.zw + bf2(be0.y), se[1], bf2(xe0.y)),
                        gate(sb.xy + bf2(be0.z), se[2], bf2(xe0.z)), gate(sb.zw + bf2(be0.w), se[3], bf2(xe0.w))};
        st128(ov, p.out, ve0 + so_x, 0, 16);
      } else {
        sfor<0, 8>([&](auto K) {
          constexpr int k = K.v;
          o0 = mfma32(W[k >> 1][2 * (k & 1)], H[14 + k], o0);
          o1 = mfma32(W[k >> 1][2 * (k & 1) + 1], H[14 + k], o1);
          SB();
          if constexpr (k == XB) { STAMP(22); BAR(); STAMP(23); SB(); }
          if constexpr (k == 5) { set_pw(65536 + w * 8192 + l31 * 256); SB(); }
        });
        sfor<8, 14>([&](auto K) { o0 = mfma32(W[K.v >> 1][2 * (K.v & 1)], H[14 + K.v], o0); SB(); });
        xl = bf2(xe1.x); xh = bf2(xe1.y); bl = bf2(be1.x); bh = bf2(be1.y);
        OPQ(xl); OPQ(xh); OPQ(bl); OPQ(bh);
        sfor<8, 14>([&](auto K) { o1 = mfma32(W[K.v >> 1][2 * (K.v & 1) + 1], H[14 + K.v], o1); if constexpr (K.v == 9) put_o(o0, 0); SB(); });
        STAMP(14);
        put_o(o1, 1);
      }
      const unsigned row = tid >> 4, c = l15 >> 1;
      unsigned pa0 = 65536 + row * 256 + (((c + 8 * (l15 & 1)) ^ (row & 7) ^ ((row & 2) << 2)) << 4);
      unsigned sr = SBUF + l15 * 512 + (((8 + (row >> 2)) ^ c) << 4) + (row & 3) * 4;
      OPQ(pa0); OPQ(sr);
      BAR();
      STAMP(15);
      f4 a[8];
#pragma unroll
      for (int k = 0; k < 8; ++k) a[k] = *lp<f4>(pa0 + k * 8192);
      const unsigned se0 = *lp<unsigned>(sr), se1 = *lp<unsigned>(sr + 256);
      SB();
      const f4 sa = ((a[0] + a[1]) + (a[2] + a[3])) + ((a[4] + a[5]) + (a[6] + a[7]));
      st64(v2u{gate(sa.xy + bl, se0, xl), gate(sa.zw + bh, se1, xh)}, p.out, ve1, so_x, 16);
      STAMP(16);
    };

    if (w < 4) {
      const unsigned ta = w & 1, ha = w >> 1;
      dma_setup();
      unsigned vb = l31 * 2, sb1 = sl * 256 + ha * 128; OPQ(vb); OPQS(sb1);
      SB();
      w1_base();
      asm volatile("s_barrier" ::: "memory");
#pragma unroll
      for (int j = 0; j < 8; ++j) dmaD(0, j, false);
      unsigned pb1a, pb1b;
      v2u Ev;
      ALD_B64S(Ev, zero, p.ws, 0u);
      ALD_U16(pb1a, vb, p.b1, sb1);
      asm volatile("buffer_load_ushort %0, %1, %2, %3 offen offset:64" : "=v"(pb1b) : "v"(vb), "s"(p.b1), "s"(sb1) : "memory");
#pragma unroll
      for (int j = 0; j < 8; ++j) dmaD(1, j, false);
      SB();
      unsigned aB[2][4], aA[2][2];
      {
        const unsigned qa = l15 >> 2, pp = lane & 3, blk = q4 & 1;
        const unsigned b0 = 32768 + (4 * ta + (l31 >> 4)) * 4096 + l15 * 128 + ((hf ^ ((l31 >> 1) & 7)) << 4);
        aA[0][0] = 32768 + 2048 + hf * 4096 + qa * 256 + (((8 * ha + 2 * blk + (pp >> 1)) ^ (qa << 2)) << 4) + 8 * (pp & 1);
        aA[0][1] = aA[0][0] ^ 64; aA[1][0] = aA[0][0] + 65536; aA[1][1] = aA[0][1] + 65536;
        OPQ(aA[0][0]); OPQ(aA[0][1]); OPQ(aA[1][0]); OPQ(aA[1][1]);
#pragma unroll
        for (int i = 0; i < 4; ++i) { aB[0][i] = b0 ^ (i << 5); aB[1][i] = aB[0][i] + 65536; OPQ(aB[0][i]); OPQ(aB[1][i]); }
      }
      w2_setup(32768);
      unsigned vst = l31 * 16, sst = WS_ST + (tt * 128 + ta * 64) * 16, vcs = l31 * 8, scs = WS_CS + (sl * 128 + ha * 64) * 8;
      unsigned vh = WS_H + ((4 * tt + 2 * ta) * 112 + 8 * sl + 4 * ha) * 1024 + lane * 16;
      OPQ(vst); OPQS(sst); OPQ(vcs); OPQS(scs); OPQ(vh);
      SB();
#pragma unroll
      for (int j = 0; j < 8; ++j) dmaD(2, j, false);
      STAMP(1);
      f16v acc[2][2];
#pragma unroll
      for (int i = 0; i < 16; ++i) { acc[0][0][i] = 0.f; acc[0][1][i] = 0.f; acc[1][0][i] = 0.f; acc[1][1][i] = 0.f; }
      v4u st0, st1;
      v2u csa, csb;
      u64 tag;
      float av[2];
      v4u W[7][4], H[28];
      {
        v4u fB[2][2][4], fA[2][2][4];
        auto rdB = [&](int i, int t, int k) { fB[i & 1][t][k] = *lp<v4u>(aB[slot_of(i) >= 98304][k] + ((slot_of(i) - 32768) & 32768) + t * 8192); };
        auto rdA = [&](int i, int m, int k) { fA[i & 1][m][k] = trfrag(aA[slot_of(i) >= 98304][m] + ((slot_of(i) - 32768) & 32768) + k * 8192); };
        auto mm = [&](int i, int t, int m, int k) { acc[t][m] = mfma32(fA[i & 1][m][k], fB[i & 1][t][k], acc[t][m]); };
#pragma unroll
        for (int i = 0; i < 7; ++i) {
          if (i == 0) asm volatile("s_waitcnt vmcnt(16)\n\ts_barrier" : "+v"(Ev), "+v"(pb1a), "+v"(pb1b) :: "memory");
          else if (i == 1) asm volatile("s_waitcnt vmcnt(8) lgkmcnt(0)\n\ts_barrier" ::: "memory");
          else if (i == 2) asm volatile("s_waitcnt vmcnt(0) lgkmcnt(0)\n\ts_barrier" ::: "memory");
          else BAR();
          if (i == 0) STAMP(3);
          if (i == 1) STAMP(4);
          if (i == 3) STAMP(19);
          if (i == 5) STAMP(2);
          SB();
#pragma unroll
          for (int n = 0; n < 16; ++n) {
            if (i > 0) mm(i - 1, (n >> 1) & 1, n & 1, n >> 2);
            SB();
            if (n < 12) { if (n % 3 == 0) rdA(i, 0, n / 3); else if (n % 3 == 1) { rdB(i, 0, n / 3); rdB(i, 1, n / 3); } else rdA(i, 1, n / 3); }
            if (i == 5 && n == 12) ALD_B128S(st0, vst, p.ws, sst);
            if (i == 5 && n == 14) asm volatile("buffer_load_dwordx4 %0, %1, %2, %3 offen offset:512 sc1" : "=v"(st1) : "v"(vst), "s"(p.ws), "s"(sst) : "memory");
            if (i == CSI && n == 13) ALD_B64S(csa, vcs, p.ws, scs);
            if (i == CSI && n == 15) asm volatile("buffer_load_dwordx2 %0, %1, %2, %3 offen offset:256 sc1" : "=v"(csb) : "v"(vcs), "s"(p.ws), "s"(scs) : "memory");
            SB();
          }
          if (i == 0) tag = (((u64)(unsigned)__builtin_amdgcn_readfirstlane(Ev.y) << 32) | (unsigned)__builtin_amdgcn_readfirstlane(Ev.x)) + 1;
        }
        STAMP(21);
        mm(6, 0, 0, 0); mm(6, 0, 1, 0); mm(6, 1, 0, 0); mm(6, 1, 1, 0); mm(6, 0, 0, 1); mm(6, 0, 1, 1); mm(6, 1, 0, 1); mm(6, 1, 1, 1);
        mm(6, 0, 0, 2); mm(6, 0, 1, 2); mm(6, 1, 0, 2); mm(6, 1, 1, 2);
        SB();
        asm volatile("s_waitcnt vmcnt(0)" : "+v"(st0), "+v"(st1), "+v"(csa), "+v"(csb) :: "memory");
        STAMP(5);
        const unsigned t32 = (unsigned)tag;
        auto stale = [&] { return __builtin_amdgcn_ballot_w64(((st0.y ^ t32) | (st0.w ^ t32) | (st1.y ^ t32) | (st1.w ^ t32) | (csa.y ^ t32) | (csb.y ^ t32)) != 0) != 0; };
        if (__builtin_expect(stale(), 0)) {
#pragma unroll 1
          for (unsigned n = 0; n < SPIN_CAP; ++n) {
            asm volatile("buffer_load_dwordx4 %0, %4, %6, %7 offen sc1\n\tbuffer_load_dwordx4 %1, %4, %6, %7 offen offset:512 sc1\n\t"
                         "buffer_load_dwordx2 %2, %5, %6, %8 offen sc1\n\tbuffer_load_dwordx2 %3, %5, %6, %8 offen offset:256 sc1\n\ts_waitcnt vmcnt(0)"
                         : "=&v"(st0), "=&v"(st1), "=&v"(csa), "=&v"(csb) : "v"(vst), "v"(vcs), "s"(p.ws), "s"(sst), "s"(scs) : "memory");
            if (!stale()) break;
            __builtin_amdgcn_s_sleep(NAP0);
          }
        }
        mm(6, 0, 0, 3); mm(6, 0, 1, 3); mm(6, 1, 0, 3); mm(6, 1, 1, 3);
        const float B0 = asf(hf ? st0.z : st0.x), B1 = asf(hf ? st1.z : st1.x);
        const float A0 = hf ? bflo(pb1a) : asf(csa.x), A1 = hf ? bflo(pb1b) : asf(csb.x);
        av[0] = __builtin_amdgcn_rcpf(asf(st0.z)); av[1] = __builtin_amdgcn_rcpf(asf(st1.z));
        acc[0][0] = __builtin_amdgcn_mfma_f32_32x32x2f32(A0, B0, acc[0][0], 0, 0, 0);
        acc[0][1] = __builtin_amdgcn_mfma_f32_32x32x2f32(A1, B0, acc[0][1], 0, 0, 0);
        acc[1][0] = __builtin_amdgcn_mfma_f32_32x32x2f32(A0, B1, acc[1][0], 0, 0, 0);
        acc[1][1] = __builtin_amdgcn_mfma_f32_32x32x2f32(A1, B1, acc[1][1], 0, 0, 0);
        SB();
        STAMP(6);
        const v2u tg2 = {(unsigned)tag, (unsigned)(tag >> 32)};
        f2 gc1 = {-0.10294324f, -0.10294324f}, gc2 = {-2.3022082f, -2.3022082f};
        OPQ(gc1); OPQ(gc2);
        sfor<0, 8>([&](auto G) {
          constexpr int g = G.v, t = g >> 2, m = (g >> 1) & 1;
          v4u o;
          f2 v[4], z[4];
#pragma unroll
          for (int j = 0; j < 4; ++j) { const int b = 8 * (g & 1) + 2 * j; v[j] = f2{acc[t][m][b], acc[t][m][b + 1]} * av[t]; }
          GSB();
#pragma unroll
          for (int j = 0; j < 4; ++j) z[j] = v[j] * v[j];
          GSB();
#pragma unroll
          for (int j = 0; j < 4; ++j) z[j] = fma2(z[j], gc1, gc2);
          GSB();
#pragma unroll
          for (int j = 0; j < 4; ++j) z[j] *= v[j];
          GSB();
#pragma unroll
          for (int j = 0; j < 4; ++j) z[j] = f2{__builtin_amdgcn_exp2f(z[j].x), __builtin_amdgcn_exp2f(z[j].y)};
          GSB();
#pragma unroll
          for (int j = 0; j < 4; ++j) z[j] += 1.0f;
          GSB();
#pragma unroll
          for (int j = 0; j < 4; ++j) z[j] = f2{__builtin_amdgcn_rcpf(z[j].x), __builtin_amdgcn_rcpf(z[j].y)};
          GSB();
#pragma unroll
          for (int j = 0; j < 4; ++j) z[j] *= v[j];
          GSB();
#pragma unroll
          for (int j = 0; j < 4; ++j) o[j] = pkbf(z[j]);
          if constexpr (g == 4) { vh += 112 * 1024; OPQ(vh); }
          st128(o, p.ws, vh + (g & 3) * 1024, 0, 16);
          SB();
          if constexpr (g == 7) { VMW(4); if (lane == 0) st64(tg2, p.ws, 0, WS_FH + (4 * tt + 2 * ta) * 4096 + (2 * sl + ha) * 128, 16); SB(); }
        });
        STAMP(7);
        asm volatile("s_barrier" ::: "memory");
        STAMP(9);
      }
      auto dmaW4 = [&](int i) { dmaW(i, 0, wstg_of(i)); dmaW(i, 1, 0); dmaW(i, 2, 0); dmaW(i, 3, 0); };
      auto rdW = [&](int i) { readW(W, i, wstg_of(i) - 32768); };
      dmaW4(0); dmaW4(1);
      VMW(9);
      if (lane == 0) st64(v2u{(unsigned)tag, (unsigned)(tag >> 32)}, p.ws, 0, WS_FH + (4 * tt + 2 * ta + 1) * 4096 + (2 * sl + ha) * 128, 16);
      STAMP(8);
      dmaW4(2); dmaW4(3);
      unsigned sh0 = WS_H + (2 * tb * 112 + 14 * w) * 1024, vl = lane * 16;
      unsigned vq = min(((14 * w) >> 2) + (lane >> 3), (14 * w + 13) >> 2) * 128;
      OPQS(sh0); OPQ(vl); OPQ(vq);
      auto loadH = [&](int n) { const int half = n / 14, k = n % 14; H[n] = ld128(p.ws, vl + (k & 3) * 1024, sh0 + half * 112 * 1024 + (k >> 2) * 4096, 16); };
      v2u hf0, hf1;
      VMW(9); rdW(0); rdW(1); LGK0();
      ALD_B64S(hf0, vq, p.ws, WS_FH + tb * 8192);
      dmaW4(4); dmaW4(5);
      VMW(9); rdW(2); rdW(3); LGK0(); dmaW4(6);
      asm volatile("s_waitcnt vmcnt(12)" : "+v"(hf0) :: "memory");
      await64(hf0, p.ws, vq, WS_FH + tb * 8192, tag);
      STAMP(18);
      sfor<0, 6>([&](auto N) { loadH(N.v); });
      ALD_B64S(hf1, vq, p.ws, WS_FH + tb * 8192 + 4096);
      VMW(7); rdW(4); rdW(5); rdW(6);
      sfor<6, 14>([&](auto N) { loadH(N.v); });
      asm volatile("s_waitcnt vmcnt(8)" : "+v"(hf1) :: "memory");
      await64(hf1, p.ws, vq, WS_FH + tb * 8192 + 4096, tag);
      STAMP(20);
      sfor<14, NHA>([&](auto N) { loadH(N.v); });
      STAMP(10);
      tail(IC<0>{}, W, H, IC<NHA>{}, loadH, tag);
    } else {
      const unsigned ch = wq >> 1, nh = wq & 1, t8 = tid & 255;
      unsigned vp = t8 * 4, sp = cp * 1024, vc = lane * 8, sc = sp + ch * 512, sdt = cp * 4 + ch * 2;
      OPQ(vp); OPQ(vc); OPQS(sp); OPQS(sc); OPQS(sdt);
      SB();
      unsigned plA, pAi, pCr, pCi, pdt;
      v2u Ev, mCr, mCi;
      ALD_U16(pdt, zero, p.ldt, sdt);
      ALD_B32(plA, vp, p.lA, sp);
      ALD_B32(pAi, vp, p.Ai, sp);
      ALD_B32(pCr, vp, p.Cr, sp);
      ALD_B32(pCi, vp, p.Ci, sp);
      ALD_B64(mCr, vc, p.Cr, sc);
      ALD_B64(mCi, vc, p.Ci, sc);
      ALD_B64S(Ev, zero, p.ws, 0u);
      asm volatile("s_barrier" ::: "memory");
      SB();
      dma_setup(); w1_base(); w2_setup(65536);
      auto bsync = [&](unsigned n) {
        LGK0();
        if (lane == 0) __hip_atomic_fetch_add(lp<unsigned>(BSYN), 1u, __ATOMIC_RELAXED, __HIP_MEMORY_SCOPE_WORKGROUP);
        bwait(BSYN, 4 * n);
      };
      sxB = sxA - (rot ? 7 * 128 : 0); swB = swA - (rot ? 7 * 64 * 3584 : 0); OPQS(sxB); OPQS(swB);
      unsigned km0 = wq * KSCR, kfa = km0 + l15 * KROW + q4 * 16; OPQS(km0); OPQ(kfa);
      unsigned tw[3], tr[9];
      {
        const unsigned tb0 = TTAB + 1536 * ch;
        tw[0] = tb0 + 512 * nh + 256 * hf + (nh ? 128u : 8u) * (q4 & 1) + (l15 << (nh ? 3 - hf : 4u));
        tw[1] = tw[0] + ((nh & hf) ? 64u : 4u);
        tw[2] = tb0 + 1024 + 128 * q4 + 2 * l15;
        const unsigned g = 255 - t8;
        const unsigned rL = wq < 2 ? 3u : wq == 2 ? 2u : 1u, rH = wq < 2 ? 3u : wq == 2 ? 2u : 0u, rho = hf ? rH : rL;
        const unsigned oL = wq < 2 ? 0u : wq == 2 ? 0x02000000u : 0x04020000u, oH = wq == 3 ? 0x06040200u : oL;
        const unsigned pL = wq < 2 ? 0x02020200u : wq == 2 ? 0x04040202u : 0x08060404u, pH = wq == 3 ? 0x0e0c0a08u : pL;
        unsigned olo = hf ? oH : oL, ohi = (hf ? pH : pL) | ((2 * g) & (wq < 2 ? 2u : 0u));
        OPQ(olo); OPQ(ohi);
        const unsigned ta = TTAB + (rho << 8) + ((16 * g) >> rho);
#pragma unroll
        for (int e = 0; e < 8; ++e) tr[7 - e] = ta + (((e < 4 ? olo : ohi) >> (8 * (e & 3))) & 0xffu);
        tr[8] = ta - (g <= 32 ? 2u : 0u);
#pragma unroll
        for (int e = 0; e < 3; ++e) OPQ(tw[e]);
#pragma unroll
        for (int e = 0; e < 9; ++e) OPQ(tr[e]);
      }
      SB();
      asm volatile("s_waitcnt vmcnt(0)" : "+v"(pdt), "+v"(plA), "+v"(pAi), "+v"(pCr), "+v"(pCi), "+v"(mCr), "+v"(mCi), "+v"(Ev) :: "memory");
      STAMP(1);
      const u64 tag = (((u64)(unsigned)__builtin_amdgcn_readfirstlane(Ev.y) << 32) | (unsigned)__builtin_amdgcn_readfirstlane(Ev.x)) + 1;
      f2 w8[2];
      z2 w1z, w128, bs;
      float ysc;
      {
        constexpr float L2E = 1.4426950408889634f;
        const float xd = bflo(pdt), td = xd * L2E;
        const float ed = __builtin_amdgcn_exp2f(td), rd = __builtin_fmaf(xd, 1.925963033500011e-8f, __builtin_fmaf(xd, L2E, -td));
        const float dt = rbf(__builtin_fmaf(ed, rd * 0.6931471805599453f, ed));
        const f2 la = bf2(plA) * L2E;
        const f2 l2 = bf2(pkbf(f2{__builtin_amdgcn_exp2f(la.x), __builtin_amdgcn_exp2f(la.y)})) * -dt * L2E;
        const f2 th = bf2(pAi) * dt * 0.15915494309189535f;
        us2 mx = __builtin_elementwise_max(__builtin_elementwise_max(__builtin_bit_cast(us2, mCr.x & 0x7fff7fffu), __builtin_bit_cast(us2, mCr.y & 0x7fff7fffu)),
                                           __builtin_elementwise_max(__builtin_bit_cast(us2, mCi.x & 0x7fff7fffu), __builtin_bit_cast(us2, mCi.y & 0x7fff7fffu)));
        unsigned ew = wave_maxu(max((unsigned)mx.x, (unsigned)mx.y)) >> 7;
        ew = ew < 1u ? 1u : ew > 253u ? 253u : ew;
        ysc = 2.f * dt * asf(ew << 23);
        const float inv = asf((254u - ew) << 23);
        auto cexp = [&](int a, float s) {
          const float mg = __builtin_amdgcn_exp2f(l2[a] * s), f = __builtin_amdgcn_fractf(th[a] * s);
          return f2{__builtin_amdgcn_cosf(f), __builtin_amdgcn_sinf(f)} * mg;
        };
        w8[0] = cexp(0, 8.f); w8[0].y = -w8[0].y; w8[1] = cexp(1, 8.f); w8[1].y = -w8[1].y;
        const f2 a0 = cexp(0, 1.f), a1 = cexp(1, 1.f), b0 = cexp(0, 128.f), b1 = cexp(1, 128.f), cr = bf2(pCr) * inv, ci = bf2(pCi) * inv;
        w1z = z2{a0.x, a0.y, a1.x, a1.y}; w128 = z2{b0.x, b0.y, b1.x, b1.y}; bs = z2{cr.x, ci.x, cr.y, ci.y};
        OPQ(w8[0]); OPQ(w8[1]);
        asm volatile("" : "+v"(w1z.r0), "+v"(w1z.i0), "+v"(w1z.r1), "+v"(w1z.i1), "+v"(w128.r0), "+v"(w128.i0), "+v"(w128.r1), "+v"(w128.i1),
                     "+v"(bs.r0), "+v"(bs.i0), "+v"(bs.r1), "+v"(bs.i1));
        OPQ(ysc);
      }
      unsigned tL[2][16], t0[2][16], t1[2][16], t2[2][16];
      v4u Bf[2][4], Af[2][4];
      f4 kc[3];
      auto frags = [&](v4u (&f)[4]) {
#pragma unroll
        for (int s = 0; s < 4; ++s) f[s] = *lp<v4u>(kfa + s * 64);
      };
      auto kmm = [&](int t) {
        f4 c0 = {0.f, 0.f, 0.f, 0.f};
#pragma unroll
        for (int s = 0; s < 4; ++s) { c0 = mfma16h(Af[0][s], Bf[0][s], c0); c0 = mfma16h(Af[1][s], Bf[1][s], c0); }
        kc[t] = c0; OPQ(kc[t]);
      };
      f2 lu[2], lv[2];
      cm m16[2];
      auto stepL = [&](int j) {
#pragma unroll
        for (int a = 0; a < 2; ++a) {
          if (j == 0) { lu[a] = cmul(w8[a], mkcm(w8[a])); m16[a] = mkcm(lu[a]); lv[a] = w8[a]; tL[a][0] = 0x3c00u; tL[a][1] = pkh(lv[a]); }
          else { lv[a] = cmul(lv[a], m16[a]); tL[a][2 * j] = pkh(lu[a]); tL[a][2 * j + 1] = pkh(lv[a]); if (j < 7) lu[a] = cmul(lu[a], m16[a]); }
        }
        OPQ(tL[0][2 * j]); OPQ(tL[1][2 * j]); OPQ(tL[0][2 * j + 1]); OPQ(tL[1][2 * j + 1]);
      };
      z2 u, v, w4z;
      auto stepH = [&](auto N) {
        constexpr int n = N.v;
        if constexpr (n == 0) { u = bs; zmul(bs, w128); }
        else if constexpr (n == 1) { v = bs; zmul(bs, w128); }
        else if constexpr (n < 16) { constexpr int j = (n - 2) >> 1; if constexpr (n & 1) zstep(v, w1z, t0[0][8 + j], t0[1][8 + j]); else zstep(u, w1z, t0[0][j], t0[1][j]); }
        else if constexpr (n == 16) { zpack(u, t0[0][7], t0[1][7]); zpack(v, t0[0][15], t0[1][15]); w4z = w1z; zmul(w1z, w4z); }
        else if constexpr (n == 17) { w4z = w1z; zmul(w4z, w1z); }
        else if constexpr (n == 18) { u = bs; zmul(bs, w128); }
        else if constexpr (n == 19) { v = bs; zmul(bs, w128); }
        else if constexpr (n < 26) { constexpr int j = (n - 20) >> 1; if constexpr (n & 1) zstep(v, w1z, t1[0][4 + j], t1[1][4 + j]); else zstep(u, w1z, t1[0][j], t1[1][j]); }
        else if constexpr (n == 26) { zpack(u, t1[0][3], t1[1][3]); zpack(v, t1[0][7], t1[1][7]); }
        else if constexpr (n < 35) { constexpr int j = (n - 27) >> 1; if constexpr (n & 1) zleaf(bs, w4z, t1[0][9 + 2 * j], t1[1][9 + 2 * j]); else zstep(bs, w128, t1[0][8 + 2 * j], t1[1][8 + 2 * j]); }
        else if constexpr (n < 43) zstep(bs, w128, t2[0][n - 35], t2[1][n - 35]);
        else zpack(bs, t2[0][8], t2[1][8]);
        SB();
      };
      v4u W[7][4], H[28];
      stepL(0); stepL(1); stepL(2); stepL(3); stepL(4); stepL(5); stepL(6); stepL(7);
      STAMP(8);
      asm volatile("s_barrier" ::: "memory");
      STAMP(3);
      sfor<0, 6>([&](auto J) { if constexpr (J.v < 4) { dmaD(3, 2 * J.v, false); dmaD(3, 2 * J.v + 1, false); SB(); } stepH(IC<0 + J.v>{}); });
      asm volatile("s_barrier" ::: "memory");
      STAMP(4);
      auto segD = [&](auto C, auto U) { sfor<0, UPS>([&](auto J) { if constexpr (J.v < 4) { dmaD(C.v, 2 * J.v, false); dmaD(C.v, 2 * J.v + 1, false); SB(); } stepH(IC<U.v + J.v>{}); }); };
      auto segW = [&](auto I, auto U) { sfor<0, UPS>([&](auto J) { if constexpr (J.v < 4) { dmaW(I.v + J.v / 2, 2 * (J.v & 1), 65536 + 16384 * (I.v + J.v / 2)); dmaW(I.v + J.v / 2, 2 * (J.v & 1) + 1, 0); SB(); } stepH(IC<U.v + J.v>{}); }); };
      *lp<unsigned>(BSYN) = 0;
      segD(IC<4>{}, IC<6>{});
      asm volatile("s_barrier" ::: "memory");
      segD(IC<5>{}, IC<6 + UPS>{});
      asm volatile("s_waitcnt vmcnt(16)\n\ts_barrier" ::: "memory");
      STAMP(19);
      segD(IC<6>{}, IC<6 + 2 * UPS>{});
      asm volatile("s_waitcnt vmcnt(16) lgkmcnt(0)\n\ts_barrier" ::: "memory");
      segW(IC<0>{}, IC<6 + 3 * UPS>{});
      asm volatile("s_waitcnt vmcnt(16)\n\ts_barrier" ::: "memory");
      STAMP(2);
      segW(IC<2>{}, IC<6 + 4 * UPS>{});
      asm volatile("s_waitcnt vmcnt(16)\n\ts_barrier" ::: "memory");
      STAMP(21);
      segW(IC<4>{}, IC<6 + 5 * UPS>{});
      VMW(16); readW(W, 0, 0); readW(W, 1, 16384);
      stepH(IC<6 + 6 * UPS>{});
      LGK0();
      dmaW(6, 0, 65536); dmaW(6, 1, 0); dmaW(6, 2, 0); dmaW(6, 3, 0);
      unsigned vf = (wq * 8 + (lane >> 3)) * 128, junk = 0; OPQ(vf);
      if (MTCH) junk = ld32(p.w2, lane < 56 ? lane * 128 : OOB, ((idx - 4) * 4 + wq) * 7168, 0);
      SETM0(km0);
      SB();
      sfor<7 + 6 * UPS, 44>([&](auto N) { stepH(N); });
      auto put = [&](unsigned (&t)[2][16], v4u (&f)[2][4], bool full) {
        kputA(t[0]); if (full) kputB(t[0]); frags(f[0]);
        kputA(t[1]); if (full) kputB(t[1]); frags(f[1]);
      };
      __builtin_amdgcn_s_setprio(BPRIO);
      v2u xf;
      ALD_B64S(xf, vf, p.ws, WS_FX);
      STAMP(5);
      put(tL, Bf, true); put(t0, Af, true);
      asm volatile("s_waitcnt vmcnt(0)" : "+v"(xf) :: "memory");
      OPQ(junk);
      await64(xf, p.ws, vf, WS_FX, tag);
      unsigned vu = (t8 >> 3) * XNS + (t8 & 7) * 32; OPQ(vu);
      const v4u ua = ld128(p.ws, vu, WS_XN + cp * 256, 16), ub = ld128(p.ws, vu + 16, WS_XN + cp * 256, 16);
      const unsigned dskp = ld32(p.Dsk, 0, cp * 4, 0);
      SB();
      kmm(0); put(t1, Af, true); readW(W, 6, 0);
      kmm(1); put(t2, Af, false); readW(W, 2, 32768);
      *lp<f2>(YSC + ch * 8) = f2{ysc, 0.f};
      kmm(2);
      {
        unsigned pwa = km0 + lane * 16; OPQ(pwa);
        *lp<f4>(pwa) = nh ? kc[0] : kc[1]; *lp<f4>(pwa + 1024) = kc[2];
        kc[0] = nh ? kc[1] : kc[0]; OPQ(kc[0]);
      }
      readW(W, 3, 49152);
      STAMP(6);
      bsync(1);
      {
        unsigned pra = (wq ^ 1) * KSCR + lane * 16; OPQ(pra);
        const f4 a = kc[0] + *lp<f4>(pra), b = kc[2] + *lp<f4>(pra + 1024);
        const unsigned h0 = pkh(a.xy), h1 = pkh(a.zw), h2_ = pkh(b.xy), h3 = pkh(b.zw);
        *lp<unsigned>(tw[0]) = h0; *lp<unsigned>(tw[1]) = h1;
        *lp<u16>(tw[2]) = (u16)h2_; *lp<u16>(tw[2] + 32) = (u16)(h2_ >> 16); *lp<u16>(tw[2] + 64) = (u16)h3; *lp<u16>(tw[2] + 96) = (u16)(h3 >> 16);
        *lp<v2u>(UARR + wq * UCP + min(lane, 55u) * 8) = v2u{0, 0};
        *lp<unsigned>(UARR + (lane & 1) * 2 * UCP + 448) = 0;
        *lp<unsigned>(BSY2) = 0;
        readW(W, 4, 65536);
        STAMP(7);
        bsync(2);
        unsigned tv[2][9];
#pragma unroll
        for (int c = 0; c < 2; ++c)
#pragma unroll
          for (int e = 0; e < 9; ++e) tv[c][e] = *lp<u16>(tr[e] + c * 1536);
        *lp<unsigned>(q4 * KCP + 4096 + l15 * 4) = 0;
        unsigned kb = t8 * 16; OPQ(kb);
#pragma unroll
        for (int c = 0; c < 2; ++c) {
          unsigned r0 = tv[c][0] | tv[c][1] << 16, r1 = tv[c][2] | tv[c][3] << 16, r2 = tv[c][4] | tv[c][5] << 16, r3 = tv[c][6] | tv[c][7] << 16;
          OPQ(r0); OPQ(r1); OPQ(r2); OPQ(r3);
          *lp<v4u>(kb + (2 * c + 1) * KCP) = v4u{r0, r1, r2, r3};
          *lp<v4u>(kb + 2 * c * KCP) = v4u{__builtin_amdgcn_alignbit(r1, r0, 16), __builtin_amdgcn_alignbit(r2, r1, 16), __builtin_amdgcn_alignbit(r3, r2, 16),
                                           __builtin_amdgcn_alignbit(t8 == 255 ? 0u : tv[c][8], r3, 16)};
        }
      }
      STAMP(20);
      {
        unsigned uw = UARR + 448 + t8 * 16; OPQ(uw);
#pragma unroll
        for (int c = 0; c < 2; ++c) {
          const unsigned sel = c ? 0x07060302u : 0x05040100u;
          const unsigned a0 = __builtin_amdgcn_perm(ua.y, ua.x, sel), a1 = __builtin_amdgcn_perm(ua.w, ua.z, sel);
          const unsigned a2 = __builtin_amdgcn_perm(ub.y, ub.x, sel), a3 = __builtin_amdgcn_perm(ub.w, ub.z, sel);
          *lp<v4u>(uw + (2 * c + 1) * UCP) = v4u{a0, a1, a2, a3};
          *lp<unsigned>(uw + 2 * c * UCP + 4) = a0; *lp<v2u>(uw + 2 * c * UCP + 8) = v2u{a1, a2}; *lp<unsigned>(uw + 2 * c * UCP + 16) = a3;
        }
      }
      unsigned sh0 = WS_H + (2 * tb * 112 + 14 * w) * 1024, vl = lane * 16;
      unsigned fa = (ch * 2 + (l15 & 1)) * KCP + 2 * (2044 - 1792 - 96 - 128 * nh - 4 * (l15 >> 1) + 8 * q4), fb = fa + 8;
      unsigned ga = UARR + (ch * 2 + (l15 & 1)) * UCP + 2 * (224 - 96 - 128 * nh + 32 * (l15 >> 1) + 8 * q4);
      OPQS(sh0); OPQ(vl); OPQ(fa); OPQ(fb); OPQ(ga);
      auto loadH = [&](int n) { const int half = n / 14, k = n % 14; H[n] = ld128(p.ws, vl + (k & 3) * 1024, sh0 + half * 112 * 1024 + (k >> 2) * 4096, 16); };
      f4 ya[8];
#pragma unroll
      for (int i = 0; i < 8; ++i) ya[i] = f4{0.f, 0.f, 0.f, 0.f};
      readW(W, 5, 81920);
      STAMP(17);
      BAR();
      STAMP(9);
      unsigned vq = hf * 4096 + min(((14 * w) >> 2) + ((lane >> 2) & 7), (14 * w + 13) >> 2) * 128; OPQ(vq);
      v2u hfl;
      conv_all(fa, fb, ga, ya, [&](auto M) {
        if constexpr (M.v == 112) hfl = ld64(p.ws, vq, WS_FH + tb * 8192, 16);
      });
      STAMP(10);
      unsigned y0a = YBUF + wq * 8192 + 4 * (32 * (l15 >> 1) + ((2 * (l15 & 1) + 8 * q4) ^ (4 * (l15 >> 1)))), y0b = y0a ^ 16;
      OPQ(y0a); OPQ(y0b);
#pragma unroll
      for (int i = 0; i < 8; ++i) { *lp<f2>(y0a + i * 1024) = ya[i].xy; *lp<f2>(y0b + i * 1024) = ya[i].zw; SB(); }
      unsigned yr = YBUF + wq * 2048 + ((lane ^ (lane >> 3)) << 4), ur = UARR + UCP + 448 + wq * 1024 + lane * 8, vs = WS_S + cp * SST + wq * 4096 + lane * 16;
      OPQ(yr); OPQ(ur); OPQ(vs);
      if (NHE) await64(hfl, p.ws, vq, WS_FH + tb * 8192, tag);
      sfor<0, NHE / 2>([&](auto N) { loadH(N.v); });
      bsync(3);
      STAMP(11);
      {
        const f4 sc = *lp<f4>(YSC);
        f4 yv[2][4];
        v2u uv[2][2];
#pragma unroll
        for (int t = 0; t < 2; ++t) {
#pragma unroll
          for (int k = 0; k < 4; ++k) yv[t][k] = *lp<f4>(yr + t * 1024 + k * 8192);
          uv[t][0] = *lp<v2u>(ur + t * 512); SB(); uv[t][1] = *lp<v2u>(ur + t * 512 + 2 * UCP); SB();
        }
        sfor<NHE / 2, NHE>([&](auto N) { loadH(N.v); });
        SB();
        const float D0 = bflo(dskp), D1 = bfhi(dskp);
#pragma unroll
        for (int t = 0; t < 2; ++t) {
          float y0[4], y1[4];
#pragma unroll
          for (int i = 0; i < 4; ++i) { y0[i] = yv[t][0][i] + yv[t][1][i]; y1[i] = yv[t][2][i] + yv[t][3][i]; OPQ(y0[i]); OPQ(y1[i]); y0[i] *= sc.x; y1[i] *= sc.z; OPQ(y0[i]); OPQ(y1[i]); }
          const h2 a0 = __builtin_bit_cast(h2, uv[t][0].x + 0u), a1 = __builtin_bit_cast(h2, uv[t][0].y + 0u);
          const h2 b0 = __builtin_bit_cast(h2, uv[t][1].x + 0u), b1 = __builtin_bit_cast(h2, uv[t][1].y + 0u);
          const v4u sv = {pkbf(f2{__builtin_fmaf((float)a0.x, D0, y0[0]), __builtin_fmaf((float)b0.x, D1, y1[0])}),
                          pkbf(f2{__builtin_fmaf((float)a0.y, D0, y0[1]), __builtin_fmaf((float)b0.y, D1, y1[1])}),
                          pkbf(f2{__builtin_fmaf((float)a1.x, D0, y0[2]), __builtin_fmaf((float)b1.x, D1, y1[2])}),
                          pkbf(f2{__builtin_fmaf((float)a1.y, D0, y0[3]), __builtin_fmaf((float)b1.y, D1, y1[3])})};
          st128(v4u{sv.x, (unsigned)tag, sv.y, (unsigned)tag}, p.ws, vs + t * 2048, 0, 16);
          st128(v4u{sv.z, (unsigned)tag, sv.w, (unsigned)tag}, p.ws, vs + t * 2048 + 1024, 0, 16);
        }
      }
      STAMP(12);
      if (!NHE) await64(hfl, p.ws, vq, WS_FH + tb * 8192, tag);
      LGK0();
      if (lane == 0) __hip_atomic_fetch_add(lp<unsigned>(BSY2), 1u, __ATOMIC_RELAXED, __HIP_MEMORY_SCOPE_WORKGROUP);
      __builtin_amdgcn_s_setprio(0);
      sfor<NHE, NHB>([&](auto N) { loadH(N.v); });
      bwait(BSY2, 4);
      STAMP(18);
      tail(IC<1>{}, W, H, IC<NHB>{}, loadH, tag);
    }
  }
}

extern "C" void kernel_launch(void* const* d_in, const int* in_sizes, int n_in, void* d_out, int out_size, void* d_ws, size_t ws_size,
                              hipStream_t stream) {
  const unsigned FL = 0x00020000u;
  HostParams h = {{{d_in[1], DM * 512, FL}, {d_in[2], DM * 512, FL}, {d_in[3], DM * 512, FL}, {d_in[4], DM * 512, FL},
                   {d_ws, WS_END, FL}, {d_in[0], LT * DM * 2, FL}, {d_in[8], DM * FF * 2 + 4096, FL}, {d_in[9], FF * 2, FL},
                   {d_in[5], DM * 2, FL}, {d_in[10], FF * DM * 2, FL}, {d_in[6], DM * 2, FL}, {d_in[11], DM * 2, FL},
                   {d_out, LT * DM * 2, FL}}};
  static_assert(sizeof(HostParams) == sizeof(Params), "descriptor block layout");
  Params p;
  __builtin_memcpy(&p, &h, sizeof(p));
  s4d_mlp_gate_kernel<<<256, 512, 0, stream>>>(p);
}
```

```cpp
#define ENC 3
#define ENCR 0
#include <hip/hip_runtime.h>
#include <hip/hip_bf16.h>

typedef unsigned short u16;
typedef unsigned long long u64;
typedef int v4i __attribute__((ext_vector_type(4)));
typedef unsigned v4u __attribute__((ext_vector_type(4)));
typedef unsigned v2u __attribute__((ext_vector_type(2)));
typedef float f2 __attribute__((ext_vector_type(2)));
typedef float f4 __attribute__((ext_vector_type(4)));
typedef float f16v __attribute__((ext_vector_type(16)));
typedef short s4 __attribute__((ext_vector_type(4)));
typedef unsigned short us2 __attribute__((ext_vector_type(2)));
typedef __bf16 b8 __attribute__((ext_vector_type(8)));
typedef __bf16 b2 __attribute__((ext_vector_type(2)));
typedef _Float16 h8 __attribute__((ext_vector_type(8)));
typedef _Float16 h2 __attribute__((ext_vector_type(2)));
#define LAS __attribute__((address_space(3)))
#define DEV __device__ __forceinline__
#define SB() __builtin_amdgcn_sched_barrier(0)
#define VMW(n) asm volatile("s_waitcnt vmcnt(" #n ")" ::: "memory")
#define LGK0() asm volatile("s_waitcnt lgkmcnt(0)" ::: "memory")
#define BAR() asm volatile("s_waitcnt lgkmcnt(0)\n\ts_barrier" ::: "memory")
#define OPQ(x) asm volatile("" : "+v"(x))
#define OPQS(x) asm volatile("" : "+s"(x))

#define STAMPS 0
#define STAMPI "s_memtime"
#define STAMP(i) do { if (i == ENC) { SB(); asm volatile("s_memrealtime %0\n\ts_waitcnt lgkmcnt(0)" : "=s"(ert), "+s"(ert0) :: "memory"); SB(); } } while (0)

constexpr int LT = 2048, DM = 448, FF = 1792;
constexpr unsigned XNS = 224 * 256 + 256, SST = 16384 + 256;
constexpr unsigned WS_E = 0, WS_FX = 4096, WS_CS = 16384, WS_ST = 32768, WS_FH = 65536, WS_XN = WS_FH + 64 * 4096,
                   WS_S = WS_XN + 32 * XNS, WS_H = WS_S + 224 * SST, WS_END = WS_H + 64 * 112 * 1024, WS_DBG = 24u << 20;
constexpr unsigned KCP = 4224, KROW = 288, KSCR = 16 * KROW, TTAB = 28672, UARR = 65536, UCP = 4640, YSC = 31744, BSYN = 31760, BSY2 = 84224, YBUF = 131072, SBUF = 131072;
constexpr unsigned wstg_of(int i) { return 32768u + 65536u * ((i >> 1) & 1) + 16384u * (i & 1); }
constexpr unsigned slot_of(int i) { return 32768u + ((i + 2) & 3) * 32768u; }
#define XB 1
#define NHA 18
#define NWG 6
#define NTB 0
#define NTC 3
#define BPRIO 1
#define NRAW 28
#define NHB 18
#define GST 1
#define GSB() SB()
#define NAP0 4
#define SK 8
#define NHE 0
#define CSI 5
#define MTCH 0
#define TS0 1
#define R5 2
#define UPS 5
#define XG1 3
#define XG2 5
constexpr unsigned CODE_PF_BYTES = 27648;
constexpr unsigned SPIN_CAP = 1u << 14;
constexpr unsigned OOB = 0x70000000u;

struct Buf { const void* p; unsigned bytes, flags; };
struct HostParams { Buf b[13]; };
struct Params { v4i lA, Ai, Cr, Ci, ws, x, w1, b1, ldt, w2, Dsk, b2, out; };

__device__ v4u ld128(v4i rs, unsigned vo, unsigned so, int aux) __asm("llvm.amdgcn.raw.buffer.load.v4i32");
__device__ v2u ld64(v4i rs, unsigned vo, unsigned so, int aux) __asm("llvm.amdgcn.raw.buffer.load.v2i32");
__device__ unsigned ld32(v4i rs, unsigned vo, unsigned so, int aux) __asm("llvm.amdgcn.raw.buffer.load.i32");
__device__ void st128(v4u v, v4i rs, unsigned vo, unsigned so, int aux) __asm("llvm.amdgcn.raw.buffer.store.v4i32");
__device__ void st64(v2u v, v4i rs, unsigned vo, unsigned so, int aux) __asm("llvm.amdgcn.raw.buffer.store.v2i32");

DEV float asf(unsigned u) { return __builtin_bit_cast(float, u); }
DEV unsigned asu(float f) { return __builtin_bit_cast(unsigned, f); }
DEV float bflo(unsigned u) { return asf(u << 16); }
DEV float bfhi(unsigned u) { return asf(u & 0xffff0000u); }
DEV f2 bf2(unsigned u) { return f2{bflo(u), bfhi(u)}; }
DEV unsigned pkbf(f2 v) { return __builtin_bit_cast(unsigned, __builtin_convertvector(v, b2)); }
DEV unsigned pkh(f2 v) { return __builtin_bit_cast(unsigned, __builtin_convertvector(v, h2)); }
DEV float rbf(float a) { return bflo(pkbf(f2{a, a})); }
DEV f2 h2f(unsigned u) { h2 h = __builtin_bit_cast(h2, u); return f2{(float)h.x, (float)h.y}; }
DEV f2 fma2(f2 a, f2 b, f2 c) { return __builtin_elementwise_fma(a, b, c); }
DEV float dot2(unsigned a, unsigned b, float s) { return __builtin_amdgcn_fdot2_f32_bf16(__builtin_bit_cast(b2, a), __builtin_bit_cast(b2, b), s, false); }
template <typename T> DEV LAS T* lp(unsigned a) { return (LAS T*)(size_t)a; }
#define ALD_U16(d, vo, rs, so) asm volatile("buffer_load_ushort %0, %1, %2, %3 offen" : "=v"(d) : "v"(vo), "s"(rs), "s"(so) : "memory")
#define ALD_B32(d, vo, rs, so) asm volatile("buffer_load_dword %0, %1, %2, %3 offen" : "=v"(d) : "v"(vo), "s"(rs), "s"(so) : "memory")
#define ALD_B64(d, vo, rs, so) asm volatile("buffer_load_dwordx2 %0, %1, %2, %3 offen" : "=v"(d) : "v"(vo), "s"(rs), "s"(so) : "memory")
#define ALD_B64S(d, vo, rs, so) asm volatile("buffer_load_dwordx2 %0, %1, %2, %3 offen sc1" : "=v"(d) : "v"(vo), "s"(rs), "s"(so) : "memory")
#define ALD_B128S(d, vo, rs, so) asm volatile("buffer_load_dwordx4 %0, %1, %2, %3 offen sc1" : "=v"(d) : "v"(vo), "s"(rs), "s"(so) : "memory")
DEV v4u trfrag(unsigned a) {
  s4 lo = __builtin_amdgcn_ds_read_tr16_b64_v4i16(lp<s4>(a)), hi = __builtin_amdgcn_ds_read_tr16_b64_v4i16(lp<s4>(a + 1024));
  v2u l = __builtin_bit_cast(v2u, lo), h = __builtin_bit_cast(v2u, hi);
  return v4u{l.x, l.y, h.x, h.y};
}
DEV f16v mfma32(v4u a, v4u b, f16v c) { return __builtin_amdgcn_mfma_f32_32x32x16_bf16(__builtin_bit_cast(b8, a), __builtin_bit_cast(b8, b), c, 0, 0, 0); }
DEV f4 mfma16h(v4u a, v4u b, f4 c) { return __builtin_amdgcn_mfma_f32_16x16x32_f16(__builtin_bit_cast(h8, a), __builtin_bit_cast(h8, b), c, 0, 0, 0); }
template <int C> DEV float dppf(float v) { return asf(__builtin_amdgcn_update_dpp(0, asu(v), C, 0xf, 0xf, true)); }
DEV float sum8(float v) { v += dppf<0xB1>(v); v += dppf<0x4E>(v); v += dppf<0x141>(v); return v; }
template <int C, int RM> DEV unsigned dppu(unsigned v) { return __builtin_amdgcn_update_dpp(0, v, C, RM, 0xf, true); }
DEV unsigned wave_maxu(unsigned v) {
  v = max(v, dppu<0xB1, 0xf>(v)); v = max(v, dppu<0x4E, 0xf>(v)); v = max(v, dppu<0x141, 0xf>(v)); v = max(v, dppu<0x140, 0xf>(v));
  v = max(v, dppu<0x142, 0xa>(v)); v = max(v, dppu<0x143, 0xc>(v));
  return __builtin_amdgcn_readlane(v, 63);
}
DEV u64 as64(v2u v) { return __builtin_bit_cast(u64, v); }
template <int NAP = NAP0> DEV void await64(v2u f, v4i rs, unsigned vo, unsigned so, u64 tag) {
  if (__builtin_expect(__builtin_amdgcn_ballot_w64(as64(f) != tag) != 0, 0)) {
#pragma unroll 1
    for (unsigned n = 0; n < SPIN_CAP; ++n) {
      asm volatile("buffer_load_dwordx2 %0, %1, %2, %3 offen sc1\n\ts_waitcnt vmcnt(0)" : "=&v"(f) : "v"(vo), "s"(rs), "s"(so) : "memory");
      if (__builtin_amdgcn_ballot_w64(as64(f) != tag) == 0) break;
      __builtin_amdgcn_s_sleep(NAP);
    }
  }
}
struct cm { f2 w, r; };
DEV cm mkcm(f2 w) { return cm{w, f2{-w.y, w.x}}; }
DEV f2 cmul(f2 a, cm m) { return fma2(a.xx, m.w, a.yy * m.r); }
DEV void kputA(const unsigned (&v)[16]) {
  asm volatile("ds_write_addtid_b32 %0 offset:0\n\tds_write_addtid_b32 %1 offset:288\n\tds_write_addtid_b32 %2 offset:576\n\tds_write_addtid_b32 %3 offset:864\n\t"
               "ds_write_addtid_b32 %4 offset:1152\n\tds_write_addtid_b32 %5 offset:1440\n\tds_write_addtid_b32 %6 offset:1728\n\tds_write_addtid_b32 %7 offset:2016\n\t"
               "ds_write_addtid_b32 %8 offset:2304"
               :: "v"(v[0]), "v"(v[1]), "v"(v[2]), "v"(v[3]), "v"(v[4]), "v"(v[5]), "v"(v[6]), "v"(v[7]), "v"(v[8]) : "memory");
}
DEV void kputB(const unsigned (&v)[16]) {
  asm volatile("ds_write_addtid_b32 %0 offset:2592\n\tds_write_addtid_b32 %1 offset:2880\n\tds_write_addtid_b32 %2 offset:3168\n\tds_write_addtid_b32 %3 offset:3456\n\t"
               "ds_write_addtid_b32 %4 offset:3744\n\tds_write_addtid_b32 %5 offset:4032\n\tds_write_addtid_b32 %6 offset:4320"
               :: "v"(v[9]), "v"(v[10]), "v"(v[11]), "v"(v[12]), "v"(v[13]), "v"(v[14]), "v"(v[15]) : "memory");
}
static_assert(KROW == 288, "offsets above");
#define SETM0(x) asm volatile("s_mov_b32 m0, %0\n\ts_nop 0" :: "s"(x) : "memory")
#define DMA(vo, rs, so, imm) asm volatile("buffer_load_dwordx4 %0, %1, %2 offen offset:" #imm " lds" :: "v"(vo), "s"(rs), "s"(so) : "memory")

struct z2 { float r0, i0, r1, i1; };
DEV void zmul(z2& z, const z2& w) {
  float t0, t1, t2, t3;
  asm("v_mul_f32 %4, %1, %9\n\tv_mul_f32 %5, %1, %8\n\tv_mul_f32 %6, %3, %11\n\tv_mul_f32 %7, %3, %10\n\t"
      "v_fma_f32 %1, %0, %9, %5\n\tv_fma_f32 %0, %0, %8, -%4\n\tv_fma_f32 %3, %2, %11, %7\n\tv_fma_f32 %2, %2, %10, -%6"
      : "+v"(z.r0), "+v"(z.i0), "+v"(z.r1), "+v"(z.i1), "=&v"(t0), "=&v"(t1), "=&v"(t2), "=&v"(t3) : "v"(w.r0), "v"(w.i0), "v"(w.r1), "v"(w.i1));
}
DEV void zstep(z2& z, const z2& w, unsigned& a, unsigned& b) {
  float t0, t1, t2, t3;
  asm volatile("v_mul_f32 %4, %1, %11\n\tv_mul_f32 %5, %1, %10\n\tv_mul_f32 %6, %3, %13\n\tv_mul_f32 %7, %3, %12\n\t"
      "v_cvt_pk_f16_f32 %8, %0, %1\n\tv_cvt_pk_f16_f32 %9, %2, %3\n\t"
      "v_fma_f32 %1, %0, %11, %5\n\tv_fma_f32 %0, %0, %10, -%4\n\tv_fma_f32 %3, %2, %13, %7\n\tv_fma_f32 %2, %2, %12, -%6"
      : "+v"(z.r0), "+v"(z.i0), "+v"(z.r1), "+v"(z.i1), "=&v"(t0), "=&v"(t1), "=&v"(t2), "=&v"(t3), "=&v"(a), "=&v"(b)
      : "v"(w.r0), "v"(w.i0), "v"(w.r1), "v"(w.i1));
}
DEV void zleaf(const z2& z, const z2& w, unsigned& a, unsigned& b) {
  float t0, t1, t2, t3;
  asm volatile("v_mul_f32 %0, %7, %11\n\tv_mul_f32 %1, %7, %10\n\tv_mul_f32 %2, %9, %13\n\tv_mul_f32 %3, %9, %12\n\t"
      "v_fma_f32 %0, %6, %10, -%0\n\tv_fma_f32 %1, %6, %11, %1\n\tv_fma_f32 %2, %8, %12, -%2\n\tv_fma_f32 %3, %8, %13, %3\n\t"
      "v_cvt_pk_f16_f32 %4, %0, %1\n\tv_cvt_pk_f16_f32 %5, %2, %3"
      : "=&v"(t0), "=&v"(t1), "=&v"(t2), "=&v"(t3), "=&v"(a), "=&v"(b)
      : "v"(z.r0), "v"(z.i0), "v"(z.r1), "v"(z.i1), "v"(w.r0), "v"(w.i0), "v"(w.r1), "v"(w.i1));
}
DEV void zpack(const z2& z, unsigned& a, unsigned& b) {
  asm volatile("v_cvt_pk_f16_f32 %0, %2, %3\n\tv_cvt_pk_f16_f32 %1, %4, %5" : "=&v"(a), "=&v"(b) : "v"(z.r0), "v"(z.i0), "v"(z.r1), "v"(z.i1));
}
template <int V> struct IC { static constexpr int v = V; };
template <int B, int E, typename FN> DEV void sfor(FN&& f) { if constexpr (B < E) { f(IC<B>{}); sfor<B + 1, E>(f); } }
constexpr int cv_i(int pm) { int i = 0; while (pm >= 8 - i) { pm -= 8 - i; ++i; } return i; }
constexpr int cv_e(int pm) { int i = 0; while (pm >= 8 - i) { pm -= 8 - i; ++i; } return 7 - i - pm; }
constexpr int cv_first(int i) { return i * 8 - i * (i - 1) / 2; }
template <typename FN> DEV void conv_all(unsigned fa, unsigned fb, unsigned ga, f4 (&acc)[8], FN&& hook) {
  v4u Fv[2][8], Gv[2][8];
  auto ldF = [&](auto J, auto I) {
    v2u a = *lp<v2u>(fa + 512 * (7 - I.v) + 64 * (3 - J.v)), b = *lp<v2u>(fb + 512 * (7 - I.v) + 64 * (3 - J.v));
    Fv[J.v & 1][I.v] = v4u{a.x, a.y, b.x, b.y};
  };
  auto ldG = [&](auto J, auto E) { Gv[J.v & 1][E.v] = *lp<v4u>(ga + 512 * E.v + 64 * (3 - J.v)); };
  ldF(IC<0>{}, IC<0>{}); SB();
  sfor<0, 8>([&](auto E) { ldG(IC<0>{}, IC<7 - E.v>{}); SB(); });
  sfor<0, 144>([&](auto M) {
    constexpr int jj = M.v / 36, pm = M.v % 36, i = cv_i(pm), e = cv_e(pm);
    acc[i + e] = mfma16h(Fv[jj & 1][i], Gv[jj & 1][e], acc[i + e]);
    SB();
    constexpr int n = M.v + 8, nj = n / 36, np = n % 36;
    if constexpr (n < 144 && np == cv_first(cv_i(np))) ldF(IC<nj>{}, IC<cv_i(np)>{});
    if constexpr (jj < 3 && pm >= 11 && pm < 35 && (pm - 11) % 3 == 0) ldG(IC<jj + 1>{}, IC<7 - (pm - 11) / 3>{});
    hook(M);
    SB();
  });
}

__global__ __launch_bounds__(512, 2) void s4d_mlp_gate_kernel(Params p) {
  __shared__ __attribute__((aligned(16))) char smem[163840];
  asm volatile("" :: "v"((LAS char*)smem) : "memory");
  const unsigned tid = threadIdx.x, lane = tid & 63;
  const unsigned w = __builtin_amdgcn_readfirstlane(tid >> 6);
  const unsigned xcd = blockIdx.x & 7, idx = blockIdx.x >> 3;
  u64 ert0, ert = 0; asm volatile("s_memrealtime %0" : "=s"(ert0) :: "memory");
  {
    u64 pc; asm volatile("s_getpc_b64 %0" : "=s"(pc));
    const v4i rC = {(int)(unsigned)pc, (int)(unsigned)(pc >> 32), (int)CODE_PF_BYTES, 0x00020000};
    asm volatile("s_mov_b32 m0, %0\n\ts_nop 0\n\tbuffer_load_dword %1, %2, %3 offen lds" :: "s"(slot_of(3) + w * 4096),
                 "v"(lane < 4 ? lane * 128 : OOB), "s"(rC), "s"(((blockIdx.x & 56) + w) * 512) : "memory");
    SB();
  }
  if (idx < 4) {
    const unsigned hid = xcd * 4 + idx, tok0 = hid * 64 + w * 8, r8 = lane >> 3, part = lane & 7;
    asm volatile("" :: "s"(p.lA), "s"(p.Ai), "s"(p.Cr), "s"(p.Ci));
    const unsigned cj = (xcd >> 1) * 4 + idx, cbk = 14 * (xcd & 1) + min(cj, 13u);
    unsigned vo = r8 * 896 + part * 16, so = tok0 * 896, vo1 = r8 * 3584 + part * 16, so1 = w * 56 * 3584 + cbk * 128;
    OPQ(vo); OPQS(so); OPQ(vo1); OPQS(so1); SB();
    v4u xr[7], wr[7];
#pragma unroll
    for (int k = 0; k < 7; ++k) xr[k] = ld128(p.x, vo + k * 128, so, 0);
    const v2u Ev = ld64(p.ws, 0, 0, 16);
    SB();
    float s0 = 0.f, s1 = 0.f, q0 = 0.f, q1 = 0.f;
#pragma unroll
    for (int k = 0; k < 7; ++k) {
      s0 = dot2(xr[k].x, 0x3f803f80u, s0); q0 = dot2(xr[k].x, xr[k].x, q0); s1 = dot2(xr[k].y, 0x3f803f80u, s1); q1 = dot2(xr[k].y, xr[k].y, q1);
      s0 = dot2(xr[k].z, 0x3f803f80u, s0); q0 = dot2(xr[k].z, xr[k].z, q0); s1 = dot2(xr[k].w, 0x3f803f80u, s1); q1 = dot2(xr[k].w, xr[k].w, q1);
    }
    STAMP(1);
    const float mean = sum8(s0 + s1) * (1.0f / DM);
    const float var = fmaxf(__builtin_fmaf(-mean, mean, sum8(q0 + q1) * (1.0f / DM)), 0.f);
    const float mb = rbf(mean);
    const float sd1 = rbf(__builtin_amdgcn_sqrtf(rbf(rbf(var) + 1e-5f)));
    const float rs1 = __builtin_amdgcn_rcpf(sd1);
    const float mb2 = rbf((mean - mb) * rs1);
    const float sd2 = rbf(__builtin_amdgcn_sqrtf(rbf(rbf(var * rs1 * rs1) + 1e-5f)));
    const u64 tag = (((u64)(unsigned)__builtin_amdgcn_readfirstlane(Ev.y) << 32) | (unsigned)__builtin_amdgcn_readfirstlane(Ev.x)) + 1;
    const v2u tg2 = {(unsigned)tag, (unsigned)(tag >> 32)};
    st128(v4u{asu(-__builtin_fmaf(mb2, sd1, mb)), tg2.x, asu(sd1 * sd2), tg2.x}, p.ws, part == 0 ? WS_ST + (tok0 + r8) * 16 : OOB, 0, 16);
    STAMP(2);
    SB();
#pragma unroll
    for (int k = 0; k < 7; ++k) wr[k] = ld128(p.w1, vo1, so1 + k * 8 * 3584, 0);
    SB();
    {
      unsigned la = part * 1024 + ((((2 * w + (r8 >> 2)) ^ part) << 4) | ((r8 & 3) << 2));
      OPQ(la);
      const f2 rs2 = {rs1, rs1}, off2 = {-mb * rs1, -mb * rs1};
#pragma unroll
      for (int k = 0; k < 7; ++k)
#pragma unroll
        for (int i = 0; i < 4; ++i) *lp<unsigned>(la + k * 8192 + i * 256) = pkh(fma2(bf2(xr[k][i]), rs2, off2));
    }
    SB();
    {
      f2 c[4];
#pragma unroll
      for (int i = 0; i < 4; ++i) {
        c[i] = ((bf2(wr[0][i]) + bf2(wr[1][i])) + (bf2(wr[2][i]) + bf2(wr[3][i]))) + ((bf2(wr[4][i]) + bf2(wr[5][i])) + bf2(wr[6][i]));
      }
      auto f32 = [](float a, float b) { auto t = __builtin_amdgcn_permlane32_swap(asu(a), asu(b), false, false); return asf(t[0]) + asf(t[1]); };
      auto f16 = [](float a, float b) { auto t = __builtin_amdgcn_permlane16_swap(asu(a), asu(b), false, false); return asf(t[0]) + asf(t[1]); };
      float a = f16(f32(c[0].x, c[2].x), f32(c[1].x, c[3].x)), b = f16(f32(c[0].y, c[2].y), f32(c[1].y, c[3].y));
      a += dppf<0x128>(a); b += dppf<0x128>(b);
      *lp<f2>(61440 + w * 256 + part * 32 + (lane >> 4) * 8) = f2{a, b};
    }
    BAR();
    STAMP(3);
    if (w == 0) {
      float c[8];
#pragma unroll
      for (int k = 0; k < 8; ++k) c[k] = *lp<float>(61440 + k * 256 + lane * 4);
      st64(v2u{asu(((c[0] + c[1]) + (c[2] + c[3])) + ((c[4] + c[5]) + (c[6] + c[7]))), tg2.x}, p.ws, cj < 14 ? lane * 8 : OOB, WS_CS + cbk * 512, 16);
    }
    {
      v4u v[7];
#pragma unroll
      for (int k = 0; k < 7; ++k) v[k] = *lp<v4u>(tid * 16 + k * 8192);
      const unsigned go = WS_XN + hid * XNS + (tid >> 4) * 256 + (((tid & 15) ^ (w & 7)) << 4);
#pragma unroll
      for (int k = 0; k < 7; ++k) st128(v[k], p.ws, go + k * 8192, 0, 16);
    }
    unsigned vt = lane < 56 ? lane * 128 : OOB, st0 = (((w & 3) * 7 + (w >> 2)) * 32 + idx * 8) * 896;
    OPQ(vt); OPQS(st0);
    SETM0(65536 + w * 256);
    auto touch = [&](v4i rs, unsigned vo, unsigned so) { asm volatile("buffer_load_dword %0, %1, %2 offen lds" :: "v"(vo), "s"(rs), "s"(so) : "memory"); };
    VMW(0);
    BAR();
    STAMP(4);
    if (tid == 0) st64(tg2, p.ws, 0, WS_FX + hid * 128, 16);
    if (!MTCH) { touch(p.w2, vt, st0); touch(p.w2, vt, st0 + 2 * 32 * 896); touch(p.w2, vt, st0 + 4 * 32 * 896);
    if (w < 4) touch(p.w2, vt, st0 + 6 * 32 * 896); }
    STAMP(6);
    STAMP(7);
    if (hid == 0) {
      const unsigned vo2 = tid < 224 ? WS_FH + ((tid / 14) * 4 * 4096) + (tid % 14) * 256 : WS_FX + (tid & 31) * 128;
      if (w < 4) await64<24>(v2u{tg2.x + 1, tg2.y}, p.ws, vo2, 0u, tag);
      BAR();
      if (tid == 0) st64(tg2, p.ws, 0, WS_E, 16);
    }
    STAMP(5);
  } else {
    const unsigned ma = idx & 3, mb_ = (idx >> 2) - 1;
    const unsigned tt = 4 * (xcd >> 1) + ma, sl = 7 * (xcd & 1) + mb_, tb = 4 * xcd + ma, cb = mb_, cp = 28 * xcd + idx - 4;
    const bool rot = (ma + mb_) & 1;
    const unsigned wq = w & 3, hf = lane >> 5, l31 = lane & 31, l15 = lane & 15, q4 = lane >> 4;
    unsigned vx0, vx1, vw0, vw1, sxA, swA, sxB, swB, m0d;
    v4i rW1 = p.w1;
    auto dma_setup = [&] {
      vx0 = (lane >> 3) * 896 + (((lane & 7) ^ q4) << 4); vx1 = (vx0 ^ 64) + 8 * 896 - 1024;
      vw0 = q4 * 3584 + ((l15 ^ (q4 << 2)) << 4) + 2048; vw1 = vw0 + 4 * 3584 - 1024;
      sxA = (tt * 128 + wq * 32) * 896 + (rot ? 4 * 128 : 0); swA = wq * 16 * 3584 + sl * 256 + (rot ? 4 * 64 * 3584 : 0);
      m0d = wq * 8192;
      OPQ(vx0); OPQ(vx1); OPQ(vw0); OPQ(vw1); OPQS(sxA); OPQS(swA); OPQS(m0d);
    };
    auto w1_base = [&] { const u64 b = (((u64)(unsigned)rW1.y << 32) | (unsigned)rW1.x) - 4096; rW1.x = (int)(unsigned)b; rW1.y = (int)(unsigned)(b >> 32); };
    auto dmaD = [&](int i, int j, bool setm0) {
      const unsigned sx = (i < 3 ? sxA : sxB) + i * 128 + (j >> 2) * 16 * 896, sw = (i < 3 ? swA : swB) + i * (64 * 3584) + (j >> 2) * 8 * 3584;
      if (setm0 || (j & 3) == 0) SETM0(m0d + slot_of(i) + (j >> 2) * 4096);
      if ((j & 3) == 0) DMA(vx0, p.x, sx, 0); else if ((j & 3) == 1) DMA(vx1, p.x, sx, 1024);
      else if ((j & 3) == 2) DMA(vw0, rW1, sw, 2048); else DMA(vw1, rW1, sw, 3072);
    };
    unsigned vw2, sv0, aW[2][2];
    auto w2_setup = [&](unsigned lo) {
      vw2 = (lane >> 3) * 896 + (((lane & 7) ^ ((q4 & 1) << 2)) << 4); sv0 = w * 7 * 32 * 896 + cb * 128;
      aW[0][0] = lo + wq * 4096 + (4 * hf + (l15 >> 2)) * 128 + (((2 * (q4 & 1) + ((lane & 3) >> 1)) ^ (((l15 >> 3) & 1) << 2)) << 4) + 8 * (lane & 1);
      aW[0][1] = aW[0][0] ^ 64; aW[1][0] = aW[0][0] + 65536; aW[1][1] = aW[0][1] + 65536;
      OPQ(vw2); OPQS(sv0); OPQ(aW[0][0]); OPQ(aW[0][1]); OPQ(aW[1][0]); OPQ(aW[1][1]);
    };
    auto dmaW = [&](int i, int j, unsigned stg) {
      const unsigned s = sv0 + i * (32 * 896);
      if (j == 0) { SETM0(stg + wq * 4096); DMA(vw2, p.w2, s, 0); } else if (j == 1) DMA(vw2, p.w2, s + 6144, 1024);
      else if (j == 2) DMA(vw2, p.w2, s + 12288, 2048); else DMA(vw2, p.w2, s + 18432, 3072);
    };
    auto readW = [&](v4u (&W)[7][4], int i, unsigned off) {
#pragma unroll
      for (int f = 0; f < 4; ++f) W[i][f] = trfrag(aW[off >> 16][f & 1] + (off & 65535) + (f >> 1) * 2048);
    };
    unsigned zero = 0; OPQ(zero);

    auto bwait = [&](unsigned a, unsigned n) {
      while ((unsigned)__builtin_amdgcn_readfirstlane(__hip_atomic_load(lp<unsigned>(a), __ATOMIC_RELAXED, __HIP_MEMORY_SCOPE_WORKGROUP)) < n) {}
      asm volatile("" ::: "memory");
    };
    auto tail = [&](auto ROLE, v4u (&W)[7][4], v4u (&H)[28], auto LA, auto&& loadH, u64 tag) {
      constexpr int role = ROLE.v, la = LA.v;
      unsigned tid = threadIdx.x; OPQ(tid);
      const unsigned lane = tid & 63, hf = lane >> 5, l31 = lane & 31, l15 = lane & 15, q4 = lane >> 4;
      unsigned pw[8];
      f16v o0, o1;
      auto zero_o = [&] {
#pragma unroll
        for (int i = 0; i < 16; ++i) { o0[i] = 0.f; o1[i] = 0.f; }
      };
      auto put_o = [&](const f16v& o, int mc) {
#pragma unroll
        for (int g = 0; g < 4; ++g) *lp<f4>(pw[4 * mc + g]) = f4{o[4 * g], o[4 * g + 1], o[4 * g + 2], o[4 * g + 3]};
      };
      auto set_pw = [&](unsigned base) {
        const unsigned pb = base + (((hf * 8) ^ (l31 & 7) ^ ((l31 & 2) << 2)) << 4);
#pragma unroll
        for (int g = 0; g < 8; ++g) { pw[g] = pb ^ (g << 4); OPQ(pw[g]); }
      };
      auto gate = [&](f2 o, unsigned sv, f2 xv) {
        const f2 z = o * -1.4426950408889634f;
        const f2 d = f2{__builtin_amdgcn_exp2f(z.x), __builtin_amdgcn_exp2f(z.y)} + 1.0f;
        return pkbf(fma2(o * f2{__builtin_amdgcn_rcpf(d.x), __builtin_amdgcn_rcpf(d.y)}, bf2(sv), xv));
      };
      const unsigned so_x = tb * 64 * 896 + cb * 128;
      unsigned ve1 = (32 + (tid >> 4)) * 896 + l15 * 8; OPQ(ve1);
      v2u xe1, be1;
      v4u xe0, be0;
      unsigned ve0 = (tid >> 3) * 896 + (tid & 7) * 16;
      zero_o();
      sfor<0, 14>([&](auto K) {
        constexpr int k = K.v;
        o0 = mfma32(W[k >> 1][2 * (k & 1)], H[k], o0);
        o1 = mfma32(W[k >> 1][2 * (k & 1) + 1], H[k], o1);
        if constexpr (k + la < 28) loadH(k + la);
        if constexpr (k + la == 28) { xe1 = ld64(p.x, ve1, so_x, 0); be1 = ld64(p.b2, l15 * 8, cb * 128, 0); }
        if constexpr (role == 0 && k + la == 29) { OPQ(ve0); xe0 = ld128(p.x, ve0, so_x, 0); be0 = ld128(p.b2, (tid & 7) * 16, cb * 128, 0); }
        SB();
        if constexpr (k == 3) { set_pw((role ? 0 : 32768) + wq * 4096 + (l31 >> 4) * 16384 + l15 * 256); SB(); }
      });
      STAMP(13);
      put_o(o0, 0); put_o(o1, 1);
      zero_o();
      SB();
      f2 xl, xh, bl, bh;
      if constexpr (role == 0) {
        v4u g0, g1, g2, g3;
        unsigned vs = (w * 8 + q4) * SST + l15 * 16, ss = WS_S + cb * 32 * SST + (tb >> 2) * 2048 + (tb & 3) * 256;
        sfor<0, 14>([&](auto K) {
          constexpr int k = K.v;
          o0 = mfma32(W[k >> 1][2 * (k & 1)], H[14 + k], o0);
          o1 = mfma32(W[k >> 1][2 * (k & 1) + 1], H[14 + k], o1);
          if constexpr (k == SK) { OPQ(vs); OPQS(ss); g0 = ld128(p.ws, vs, ss, 16); g1 = ld128(p.ws, vs + 1024, ss, 16); g2 = ld128(p.ws, vs, ss + 4 * SST, 16); g3 = ld128(p.ws, vs + 1024, ss + 4 * SST, 16); }
          SB();
        });
        STAMP(14);
        {
          const unsigned t32 = (unsigned)tag;
          auto stale = [&] { return __builtin_amdgcn_ballot_w64(((g0.y ^ t32) | (g0.w ^ t32) | (g1.y ^ t32) | (g1.w ^ t32) | (g2.y ^ t32) | (g2.w ^ t32) | (g3.y ^ t32) | (g3.w ^ t32)) != 0) != 0; };
          if (__builtin_expect(stale(), 0)) {
#pragma unroll 1
            for (unsigned n = 0; n < SPIN_CAP; ++n) {
              asm volatile("buffer_load_dwordx4 %0, %4, %5, %6 offen sc1\n\tbuffer_load_dwordx4 %1, %4, %5, %6 offen offset:1024 sc1\n\t"
                           "buffer_load_dwordx4 %2, %4, %5, %7 offen sc1\n\tbuffer_load_dwordx4 %3, %4, %5, %7 offen offset:1024 sc1\n\ts_waitcnt vmcnt(0)"
                           : "=&v"(g0), "=&v"(g1), "=&v"(g2), "=&v"(g3) : "v"(vs), "s"(p.ws), "s"(ss), "s"(ss + 4 * SST) : "memory");
              if (!stale()) break;
              __builtin_amdgcn_s_sleep(NAP0);
            }
          }
        }
        const v4u sq0 = {g0.x, g0.z, g1.x, g1.z}, sq1 = {g2.x, g2.z, g3.x, g3.z};
        set_pw(65536 + w * 8192 + l31 * 256);
        const unsigned row = tid >> 3;
        unsigned pa0 = (row >> 4) * 16384 + (row & 15) * 256 + (((tid & 7) ^ (row & 7) ^ ((row & 2) << 2)) << 4), pb0 = pa0 ^ 128;
        unsigned sr = SBUF + (tid & 7) * 1024 + (((row >> 2) ^ (tid & 7)) << 4) + (row & 3) * 4;
        OPQ(pa0); OPQ(pb0); OPQ(sr);
        xl = bf2(xe1.x); xh = bf2(xe1.y); bl = bf2(be1.x); bh = bf2(be1.y);
        OPQ(xl); OPQ(xh); OPQ(bl); OPQ(bh);
        bwait(BSY2, 4);
        *lp<v4u>(SBUF + (w * 8 + q4) * 256 + ((l15 ^ (2 * w)) << 4)) = sq0;
        *lp<v4u>(SBUF + (w * 8 + 4 + q4) * 256 + ((l15 ^ (2 * w + 1)) << 4)) = sq1;
        STAMP(22);
        BAR();
        STAMP(23);
        f4 a[8], b[8];
#pragma unroll
        for (int k = 0; k < 8; ++k) { a[k] = *lp<f4>(pa0 + (k >> 2) * 32768 + (k & 3) * 4096); b[k] = *lp<f4>(pb0 + (k >> 2) * 32768 + (k & 3) * 4096); }
        unsigned se[4];
#pragma unroll
        for (int i = 0; i < 4; ++i) se[i] = *lp<unsigned>(sr + i * 256);
        put_o(o0, 0); put_o(o1, 1);
        SB();
        const f4 sa = ((a[0] + a[1]) + (a[2] + a[3])) + ((a[4] + a[5]) + (a[6] + a[7]));
        const f4 sb = ((b[0] + b[1]) + (b[2] + b[3])) + ((b[4] + b[5]) + (b[6] + b[7]));
        const v4u ov = {gate(sa.xy + bf2(be0.x), se[0], bf2(xe0.x)), gate(sa.zw + bf2(be0.y), se[1], bf2(xe0.y)),
                        gate(sb.xy + bf2(be0.z), se[2], bf2(xe0.z)), gate(sb.zw + bf2(be0.w), se[3], bf2(xe0.w))};
        st128(ov, p.out, ve0 + so_x, 0, 16);
      } else {
        sfor<0, 8>([&](auto K) {
          constexpr int k = K.v;
          o0 = mfma32(W[k >> 1][2 * (k & 1)], H[14 + k], o0);
          o1 = mfma32(W[k >> 1][2 * (k & 1) + 1], H[14 + k], o1);
          SB();
          if constexpr (k == XB) { STAMP(22); BAR(); STAMP(23); SB(); }
          if constexpr (k == 5) { set_pw(65536 + w * 8192 + l31 * 256); SB(); }
        });
        sfor<8, 14>([&](auto K) { o0 = mfma32(W[K.v >> 1][2 * (K.v & 1)], H[14 + K.v], o0); SB(); });
        xl = bf2(xe1.x); xh = bf2(xe1.y); bl = bf2(be1.x); bh = bf2(be1.y);
        OPQ(xl); OPQ(xh); OPQ(bl); OPQ(bh);
        sfor<8, 14>([&](auto K) { o1 = mfma32(W[K.v >> 1][2 * (K.v & 1) + 1], H[14 + K.v], o1); if constexpr (K.v == 9) put_o(o0, 0); SB(); });
        STAMP(14);
        put_o(o1, 1);
      }
      const unsigned row = tid >> 4, c = l15 >> 1;
      unsigned pa0 = 65536 + row * 256 + (((c + 8 * (l15 & 1)) ^ (row & 7) ^ ((row & 2) << 2)) << 4);
      unsigned sr = SBUF + l15 * 512 + (((8 + (row >> 2)) ^ c) << 4) + (row & 3) * 4;
      OPQ(pa0); OPQ(sr);
      BAR();
      STAMP(15);
      f4 a[8];
#pragma unroll
      for (int k = 0; k < 8; ++k) a[k] = *lp<f4>(pa0 + k * 8192);
      const unsigned se0 = *lp<unsigned>(sr), se1 = *lp<unsigned>(sr + 256);
      SB();
      const f4 sa = ((a[0] + a[1]) + (a[2] + a[3])) + ((a[4] + a[5]) + (a[6] + a[7]));
      st64(v2u{gate(sa.xy + bl, se0, xl), gate(sa.zw + bh, se1, xh)}, p.out, ve1, so_x, 16);
      STAMP(16);
    };

    if (w < 4) {
      const unsigned ta = w & 1, ha = w >> 1;
      dma_setup();
      unsigned vb = l31 * 2, sb1 = sl * 256 + ha * 128; OPQ(vb); OPQS(sb1);
      SB();
      w1_base();
      asm volatile("s_barrier" ::: "memory");
#pragma unroll
      for (int j = 0; j < 8; ++j) dmaD(0, j, false);
      unsigned pb1a, pb1b;
      v2u Ev;
      ALD_B64S(Ev, zero, p.ws, 0u);
      ALD_U16(pb1a, vb, p.b1, sb1);
      asm volatile("buffer_load_ushort %0, %1, %2, %3 offen offset:64" : "=v"(pb1b) : "v"(vb), "s"(p.b1), "s"(sb1) : "memory");
#pragma unroll
      for (int j = 0; j < 8; ++j) dmaD(1, j, false);
      SB();
      unsigned aB[2][4], aA[2][2];
      {
        const unsigned qa = l15 >> 2, pp = lane & 3, blk = q4 & 1;
        const unsigned b0 = 32768 + (4 * ta + (l31 >> 4)) * 4096 + l15 * 128 + ((hf ^ ((l31 >> 1) & 7)) << 4);
        aA[0][0] = 32768 + 2048 + hf * 4096 + qa * 256 + (((8 * ha + 2 * blk + (pp >> 1)) ^ (qa << 2)) << 4) + 8 * (pp & 1);
        aA[0][1] = aA[0][0] ^ 64; aA[1][0] = aA[0][0] + 65536; aA[1][1] = aA[0][1] + 65536;
        OPQ(aA[0][0]); OPQ(aA[0][1]); OPQ(aA[1][0]); OPQ(aA[1][1]);
#pragma unroll
        for (int i = 0; i < 4; ++i) { aB[0][i] = b0 ^ (i << 5); aB[1][i] = aB[0][i] + 65536; OPQ(aB[0][i]); OPQ(aB[1][i]); }
      }
      w2_setup(32768);
      unsigned vst = l31 * 16, sst = WS_ST + (tt * 128 + ta * 64) * 16, vcs = l31 * 8, scs = WS_CS + (sl * 128 + ha * 64) * 8;
      unsigned vh = WS_H + ((4 * tt + 2 * ta) * 112 + 8 * sl + 4 * ha) * 1024 + lane * 16;
      OPQ(vst); OPQS(sst); OPQ(vcs); OPQS(scs); OPQ(vh);
      SB();
#pragma unroll
      for (int j = 0; j < 8; ++j) dmaD(2, j, false);
      STAMP(1);
      f16v acc[2][2];
#pragma unroll
      for (int i = 0; i < 16; ++i) { acc[0][0][i] = 0.f; acc[0][1][i] = 0.f; acc[1][0][i] = 0.f; acc[1][1][i] = 0.f; }
      v4u st0, st1;
      v2u csa, csb;
      u64 tag;
      float av[2];
      v4u W[7][4], H[28];
      {
        v4u fB[2][2][4], fA[2][2][4];
        auto rdB = [&](int i, int t, int k) { fB[i & 1][t][k] = *lp<v4u>(aB[slot_of(i) >= 98304][k] + ((slot_of(i) - 32768) & 32768) + t * 8192); };
        auto rdA = [&](int i, int m, int k) { fA[i & 1][m][k] = trfrag(aA[slot_of(i) >= 98304][m] + ((slot_of(i) - 32768) & 32768) + k * 8192); };
        auto mm = [&](int i, int t, int m, int k) { acc[t][m] = mfma32(fA[i & 1][m][k], fB[i & 1][t][k], acc[t][m]); };
#pragma unroll
        for (int i = 0; i < 7; ++i) {
          if (i == 0) asm volatile("s_waitcnt vmcnt(16)\n\ts_barrier" : "+v"(Ev), "+v"(pb1a), "+v"(pb1b) :: "memory");
          else if (i == 1) asm volatile("s_waitcnt vmcnt(8) lgkmcnt(0)\n\ts_barrier" ::: "memory");
          else if (i == 2) asm volatile("s_waitcnt vmcnt(0) lgkmcnt(0)\n\ts_barrier" ::: "memory");
          else BAR();
          if (i == 0) STAMP(3);
          if (i == 1) STAMP(4);
          if (i == 3) STAMP(19);
          if (i == 5) STAMP(2);
          SB();
#pragma unroll
          for (int n = 0; n < 16; ++n) {
            if (i > 0) mm(i - 1, (n >> 1) & 1, n & 1, n >> 2);
            SB();
            if (n < 12) { if (n % 3 == 0) rdA(i, 0, n / 3); else if (n % 3 == 1) { rdB(i, 0, n / 3); rdB(i, 1, n / 3); } else rdA(i, 1, n / 3); }
            if (i == 5 && n == 12) ALD_B128S(st0, vst, p.ws, sst);
            if (i == 5 && n == 14) asm volatile("buffer_load_dwordx4 %0, %1, %2, %3 offen offset:512 sc1" : "=v"(st1) : "v"(vst), "s"(p.ws), "s"(sst) : "memory");
            if (i == CSI && n == 13) ALD_B64S(csa, vcs, p.ws, scs);
            if (i == CSI && n == 15) asm volatile("buffer_load_dwordx2 %0, %1, %2, %3 offen offset:256 sc1" : "=v"(csb) : "v"(vcs), "s"(p.ws), "s"(scs) : "memory");
            SB();
          }
          if (i == 0) tag = (((u64)(unsigned)__builtin_amdgcn_readfirstlane(Ev.y) << 32) | (unsigned)__builtin_amdgcn_readfirstlane(Ev.x)) + 1;
        }
        STAMP(21);
        mm(6, 0, 0, 0); mm(6, 0, 1, 0); mm(6, 1, 0, 0); mm(6, 1, 1, 0); mm(6, 0, 0, 1); mm(6, 0, 1, 1); mm(6, 1, 0, 1); mm(6, 1, 1, 1);
        mm(6, 0, 0, 2); mm(6, 0, 1, 2); mm(6, 1, 0, 2); mm(6, 1, 1, 2);
        SB();
        asm volatile("s_waitcnt vmcnt(0)" : "+v"(st0), "+v"(st1), "+v"(csa), "+v"(csb) :: "memory");
        STAMP(5);
        const unsigned t32 = (unsigned)tag;
        auto stale = [&] { return __builtin_amdgcn_ballot_w64(((st0.y ^ t32) | (st0.w ^ t32) | (st1.y ^ t32) | (st1.w ^ t32) | (csa.y ^ t32) | (csb.y ^ t32)) != 0) != 0; };
        if (__builtin_expect(stale(), 0)) {
#pragma unroll 1
          for (unsigned n = 0; n < SPIN_CAP; ++n) {
            asm volatile("buffer_load_dwordx4 %0, %4, %6, %7 offen sc1\n\tbuffer_load_dwordx4 %1, %4, %6, %7 offen offset:512 sc1\n\t"
                         "buffer_load_dwordx2 %2, %5, %6, %8 offen sc1\n\tbuffer_load_dwordx2 %3, %5, %6, %8 offen offset:256 sc1\n\ts_waitcnt vmcnt(0)"
                         : "=&v"(st0), "=&v"(st1), "=&v"(csa), "=&v"(csb) : "v"(vst), "v"(vcs), "s"(p.ws), "s"(sst), "s"(scs) : "memory");
            if (!stale()) break;
            __builtin_amdgcn_s_sleep(NAP0);
          }
        }
        mm(6, 0, 0, 3); mm(6, 0, 1, 3); mm(6, 1, 0, 3); mm(6, 1, 1, 3);
        const float B0 = asf(hf ? st0.z : st0.x), B1 = asf(hf ? st1.z : st1.x);
        const float A0 = hf ? bflo(pb1a) : asf(csa.x), A1 = hf ? bflo(pb1b) : asf(csb.x);
        av[0] = __builtin_amdgcn_rcpf(asf(st0.z)); av[1] = __builtin_amdgcn_rcpf(asf(st1.z));
        acc[0][0] = __builtin_amdgcn_mfma_f32_32x32x2f32(A0, B0, acc[0][0], 0, 0, 0);
        acc[0][1] = __builtin_amdgcn_mfma_f32_32x32x2f32(A1, B0, acc[0][1], 0, 0, 0);
        acc[1][0] = __builtin_amdgcn_mfma_f32_32x32x2f32(A0, B1, acc[1][0], 0, 0, 0);
        acc[1][1] = __builtin_amdgcn_mfma_f32_32x32x2f32(A1, B1, acc[1][1], 0, 0, 0);
        SB();
        STAMP(6);
        const v2u tg2 = {(unsigned)tag, (unsigned)(tag >> 32)};
        f2 gc1 = {-0.10294324f, -0.10294324f}, gc2 = {-2.3022082f, -2.3022082f};
        OPQ(gc1); OPQ(gc2);
        sfor<0, 8>([&](auto G) {
          constexpr int g = G.v, t = g >> 2, m = (g >> 1) & 1;
          v4u o;
          f2 v[4], z[4];
#pragma unroll
          for (int j = 0; j < 4; ++j) { const int b = 8 * (g & 1) + 2 * j; v[j] = f2{acc[t][m][b], acc[t][m][b + 1]} * av[t]; }
          GSB();
#pragma unroll
          for (int j = 0; j < 4; ++j) z[j] = v[j] * v[j];
          GSB();
#pragma unroll
          for (int j = 0; j < 4; ++j) z[j] = fma2(z[j], gc1, gc2);
          GSB();
#pragma unroll
          for (int j = 0; j < 4; ++j) z[j] *= v[j];
          GSB();
#pragma unroll
          for (int j = 0; j < 4; ++j) z[j] = f2{__builtin_amdgcn_exp2f(z[j].x), __builtin_amdgcn_exp2f(z[j].y)};
          GSB();
#pragma unroll
          for (int j = 0; j < 4; ++j) z[j] += 1.0f;
          GSB();
#pragma unroll
          for (int j = 0; j < 4; ++j) z[j] = f2{__builtin_amdgcn_rcpf(z[j].x), __builtin_amdgcn_rcpf(z[j].y)};
          GSB();
#pragma unroll
          for (int j = 0; j < 4; ++j) z[j] *= v[j];
          GSB();
#pragma unroll
          for (int j = 0; j < 4; ++j) o[j] = pkbf(z[j]);
          if constexpr (g == 4) { vh += 112 * 1024; OPQ(vh); }
          st128(o, p.ws, vh + (g & 3) * 1024, 0, 16);
          SB();
          if constexpr (g == 7) { VMW(4); if (lane == 0) st64(tg2, p.ws, 0, WS_FH + (4 * tt + 2 * ta) * 4096 + (2 * sl + ha) * 128, 16); SB(); }
        });
        STAMP(7);
        asm volatile("s_barrier" ::: "memory");
        STAMP(9);
      }
      auto dmaW4 = [&](int i) { dmaW(i, 0, wstg_of(i)); dmaW(i, 1, 0); dmaW(i, 2, 0); dmaW(i, 3, 0); };
      auto rdW = [&](int i) { readW(W, i, wstg_of(i) - 32768); };
      dmaW4(0); dmaW4(1);
      VMW(9);
      if (lane == 0) st64(v2u{(unsigned)tag, (unsigned)(tag >> 32)}, p.ws, 0, WS_FH + (4 * tt + 2 * ta + 1) * 4096 + (2 * sl + ha) * 128, 16);
      STAMP(8);
      dmaW4(2); dmaW4(3);
      unsigned sh0 = WS_H + (2 * tb * 112 + 14 * w) * 1024, vl = lane * 16;
      unsigned vq = min(((14 * w) >> 2) + (lane >> 3), (14 * w + 13) >> 2) * 128;
      OPQS(sh0); OPQ(vl); OPQ(vq);
      auto loadH = [&](int n) { const int half = n / 14, k = n % 14; H[n] = ld128(p.ws, vl + (k & 3) * 1024, sh0 + half * 112 * 1024 + (k >> 2) * 4096, 16); };
      v2u hf0, hf1;
      VMW(9); rdW(0); rdW(1); LGK0();
      ALD_B64S(hf0, vq, p.ws, WS_FH + tb * 8192);
      dmaW4(4); dmaW4(5);
      VMW(9); rdW(2); rdW(3); LGK0(); dmaW4(6);
      asm volatile("s_waitcnt vmcnt(12)" : "+v"(hf0) :: "memory");
      await64(hf0, p.ws, vq, WS_FH + tb * 8192, tag);
      STAMP(18);
      sfor<0, 6>([&](auto N) { loadH(N.v); });
      ALD_B64S(hf1, vq, p.ws, WS_FH + tb * 8192 + 4096);
      VMW(7); rdW(4); rdW(5); rdW(6);
      sfor<6, 14>([&](auto N) { loadH(N.v); });
      asm volatile("s_waitcnt vmcnt(8)" : "+v"(hf1) :: "memory");
      await64(hf1, p.ws, vq, WS_FH + tb * 8192 + 4096, tag);
      STAMP(20);
      sfor<14, NHA>([&](auto N) { loadH(N.v); });
      STAMP(10);
      tail(IC<0>{}, W, H, IC<NHA>{}, loadH, tag);
    } else {
      const unsigned ch = wq >> 1, nh = wq & 1, t8 = tid & 255;
      unsigned vp = t8 * 4, sp = cp * 1024, vc = lane * 8, sc = sp + ch * 512, sdt = cp * 4 + ch * 2;
      OPQ(vp); OPQ(vc); OPQS(sp); OPQS(sc); OPQS(sdt);
      SB();
      unsigned plA, pAi, pCr, pCi, pdt;
      v2u Ev, mCr, mCi;
      ALD_U16(pdt, zero, p.ldt, sdt);
      ALD_B32(plA, vp, p.lA, sp);
      ALD_B32(pAi, vp, p.Ai, sp);
      ALD_B32(pCr, vp, p.Cr, sp);
      ALD_B32(pCi, vp, p.Ci, sp);
      ALD_B64(mCr, vc, p.Cr, sc);
      ALD_B64(mCi, vc, p.Ci, sc);
      ALD_B64S(Ev, zero, p.ws, 0u);
      asm volatile("s_barrier" ::: "memory");
      SB();
      dma_setup(); w1_base(); w2_setup(65536);
      auto bsync = [&](unsigned n) {
        LGK0();
        if (lane == 0) __hip_atomic_fetch_add(lp<unsigned>(BSYN), 1u, __ATOMIC_RELAXED, __HIP_MEMORY_SCOPE_WORKGROUP);
        bwait(BSYN, 4 * n);
      };
      sxB = sxA - (rot ? 7 * 128 : 0); swB = swA - (rot ? 7 * 64 * 3584 : 0); OPQS(sxB); OPQS(swB);
      unsigned km0 = wq * KSCR, kfa = km0 + l15 * KROW + q4 * 16; OPQS(km0); OPQ(kfa);
      unsigned tw[3], tr[9];
      {
        const unsigned tb0 = TTAB + 1536 * ch;
        tw[0] = tb0 + 512 * nh + 256 * hf + (nh ? 128u : 8u) * (q4 & 1) + (l15 << (nh ? 3 - hf : 4u));
        tw[1] = tw[0] + ((nh & hf) ? 64u : 4u);
        tw[2] = tb0 + 1024 + 128 * q4 + 2 * l15;
        const unsigned g = 255 - t8;
        const unsigned rL = wq < 2 ? 3u : wq == 2 ? 2u : 1u, rH = wq < 2 ? 3u : wq == 2 ? 2u : 0u, rho = hf ? rH : rL;
        const unsigned oL = wq < 2 ? 0u : wq == 2 ? 0x02000000u : 0x04020000u, oH = wq == 3 ? 0x06040200u : oL;
        const unsigned pL = wq < 2 ? 0x02020200u : wq == 2 ? 0x04040202u : 0x08060404u, pH = wq == 3 ? 0x0e0c0a08u : pL;
        unsigned olo = hf ? oH : oL, ohi = (hf ? pH : pL) | ((2 * g) & (wq < 2 ? 2u : 0u));
        OPQ(olo); OPQ(ohi);
        const unsigned ta = TTAB + (rho << 8) + ((16 * g) >> rho);
#pragma unroll
        for (int e = 0; e < 8; ++e) tr[7 - e] = ta + (((e < 4 ? olo : ohi) >> (8 * (e & 3))) & 0xffu);
        tr[8] = ta - (g <= 32 ? 2u : 0u);
#pragma unroll
        for (int e = 0; e < 3; ++e) OPQ(tw[e]);
#pragma unroll
        for (int e = 0; e < 9; ++e) OPQ(tr[e]);
      }
      SB();
      asm volatile("s_waitcnt vmcnt(0)" : "+v"(pdt), "+v"(plA), "+v"(pAi), "+v"(pCr), "+v"(pCi), "+v"(mCr), "+v"(mCi), "+v"(Ev) :: "memory");
      STAMP(1);
      const u64 tag = (((u64)(unsigned)__builtin_amdgcn_readfirstlane(Ev.y) << 32) | (unsigned)__builtin_amdgcn_readfirstlane(Ev.x)) + 1;
      f2 w8[2];
      z2 w1z, w128, bs;
      float ysc;
      {
        constexpr float L2E = 1.4426950408889634f;
        const float xd = bflo(pdt), td = xd * L2E;
        const float ed = __builtin_amdgcn_exp2f(td), rd = __builtin_fmaf(xd, 1.925963033500011e-8f, __builtin_fmaf(xd, L2E, -td));
        const float dt = rbf(__builtin_fmaf(ed, rd * 0.6931471805599453f, ed));
        const f2 la = bf2(plA) * L2E;
        const f2 l2 = bf2(pkbf(f2{__builtin_amdgcn_exp2f(la.x), __builtin_amdgcn_exp2f(la.y)})) * -dt * L2E;
        const f2 th = bf2(pAi) * dt * 0.15915494309189535f;
        us2 mx = __builtin_elementwise_max(__builtin_elementwise_max(__builtin_bit_cast(us2, mCr.x & 0x7fff7fffu), __builtin_bit_cast(us2, mCr.y & 0x7fff7fffu)),
                                           __builtin_elementwise_max(__builtin_bit_cast(us2, mCi.x & 0x7fff7fffu), __builtin_bit_cast(us2, mCi.y & 0x7fff7fffu)));
        unsigned ew = wave_maxu(max((unsigned)mx.x, (unsigned)mx.y)) >> 7;
        ew = ew < 1u ? 1u : ew > 253u ? 253u : ew;
        ysc = 2.f * dt * asf(ew << 23);
        const float inv = asf((254u - ew) << 23);
        auto cexp = [&](int a, float s) {
          const float mg = __builtin_amdgcn_exp2f(l2[a] * s), f = __builtin_amdgcn_fractf(th[a] * s);
          return f2{__builtin_amdgcn_cosf(f), __builtin_amdgcn_sinf(f)} * mg;
        };
        w8[0] = cexp(0, 8.f); w8[0].y = -w8[0].y; w8[1] = cexp(1, 8.f); w8[1].y = -w8[1].y;
        const f2 a0 = cexp(0, 1.f), a1 = cexp(1, 1.f), b0 = cexp(0, 128.f), b1 = cexp(1, 128.f), cr = bf2(pCr) * inv, ci = bf2(pCi) * inv;
        w1z = z2{a0.x, a0.y, a1.x, a1.y}; w128 = z2{b0.x, b0.y, b1.x, b1.y}; bs = z2{cr.x, ci.x, cr.y, ci.y};
        OPQ(w8[0]); OPQ(w8[1]);
        asm volatile("" : "+v"(w1z.r0), "+v"(w1z.i0), "+v"(w1z.r1), "+v"(w1z.i1), "+v"(w128.r0), "+v"(w128.i0), "+v"(w128.r1), "+v"(w128.i1),
                     "+v"(bs.r0), "+v"(bs.i0), "+v"(bs.r1), "+v"(bs.i1));
        OPQ(ysc);
      }
      unsigned tL[2][16], t0[2][16], t1[2][16], t2[2][16];
      v4u Bf[2][4], Af[2][4];
      f4 kc[3];
      auto frags = [&](v4u (&f)[4]) {
#pragma unroll
        for (int s = 0; s < 4; ++s) f[s] = *lp<v4u>(kfa + s * 64);
      };
      auto kmm = [&](int t) {
        f4 c0 = {0.f, 0.f, 0.f, 0.f};
#pragma unroll
        for (int s = 0; s < 4; ++s) { c0 = mfma16h(Af[0][s], Bf[0][s], c0); c0 = mfma16h(Af[1][s], Bf[1][s], c0); }
        kc[t] = c0; OPQ(kc[t]);
      };
      f2 lu[2], lv[2];
      cm m16[2];
      auto stepL = [&](int j) {
#pragma unroll
        for (int a = 0; a < 2; ++a) {
          if (j == 0) { lu[a] = cmul(w8[a], mkcm(w8[a])); m16[a] = mkcm(lu[a]); lv[a] = w8[a]; tL[a][0] = 0x3c00u; tL[a][1] = pkh(lv[a]); }
          else { lv[a] = cmul(lv[a], m16[a]); tL[a][2 * j] = pkh(lu[a]); tL[a][2 * j + 1] = pkh(lv[a]); if (j < 7) lu[a] = cmul(lu[a], m16[a]); }
        }
        OPQ(tL[0][2 * j]); OPQ(tL[1][2 * j]); OPQ(tL[0][2 * j + 1]); OPQ(tL[1][2 * j + 1]);
      };
      z2 u, v, w4z;
      auto stepH = [&](auto N) {
        constexpr int n = N.v;
        if constexpr (n == 0) { u = bs; zmul(bs, w128); }
        else if constexpr (n == 1) { v = bs; zmul(bs, w128); }
        else if constexpr (n < 16) { constexpr int j = (n - 2) >> 1; if constexpr (n & 1) zstep(v, w1z, t0[0][8 + j], t0[1][8 + j]); else zstep(u, w1z, t0[0][j], t0[1][j]); }
        else if constexpr (n == 16) { zpack(u, t0[0][7], t0[1][7]); zpack(v, t0[0][15], t0[1][15]); w4z = w1z; zmul(w1z, w4z); }
        else if constexpr (n == 17) { w4z = w1z; zmul(w4z, w1z); }
        else if constexpr (n == 18) { u = bs; zmul(bs, w128); }
        else if constexpr (n == 19) { v = bs; zmul(bs, w128); }
        else if constexpr (n < 26) { constexpr int j = (n - 20) >> 1; if constexpr (n & 1) zstep(v, w1z, t1[0][4 + j], t1[1][4 + j]); else zstep(u, w1z, t1[0][j], t1[1][j]); }
        else if constexpr (n == 26) { zpack(u, t1[0][3], t1[1][3]); zpack(v, t1[0][7], t1[1][7]); }
        else if constexpr (n < 35) { constexpr int j = (n - 27) >> 1; if constexpr (n & 1) zleaf(bs, w4z, t1[0][9 + 2 * j], t1[1][9 + 2 * j]); else zstep(bs, w128, t1[0][8 + 2 * j], t1[1][8 + 2 * j]); }
        else if constexpr (n < 43) zstep(bs, w128, t2[0][n - 35], t2[1][n - 35]);
        else zpack(bs, t2[0][8], t2[1][8]);
        SB();
      };
      v4u W[7][4], H[28];
      stepL(0); stepL(1); stepL(2); stepL(3); stepL(4); stepL(5); stepL(6); stepL(7);
      STAMP(8);
      asm volatile("s_barrier" ::: "memory");
      STAMP(3);
      sfor<0, 6>([&](auto J) { if constexpr (J.v < 4) { dmaD(3, 2 * J.v, false); dmaD(3, 2 * J.v + 1, false); SB(); } stepH(IC<0 + J.v>{}); });
      asm volatile("s_barrier" ::: "memory");
      STAMP(4);
      auto segD = [&](auto C, auto U) { sfor<0, UPS>([&](auto J) { if constexpr (J.v < 4) { dmaD(C.v, 2 * J.v, false); dmaD(C.v, 2 * J.v + 1, false); SB(); } stepH(IC<U.v + J.v>{}); }); };
      auto segW = [&](auto I, auto U) { sfor<0, UPS>([&](auto J) { if constexpr (J.v < 4) { dmaW(I.v + J.v / 2, 2 * (J.v & 1), 65536 + 16384 * (I.v + J.v / 2)); dmaW(I.v + J.v / 2, 2 * (J.v & 1) + 1, 0); SB(); } stepH(IC<U.v + J.v>{}); }); };
      *lp<unsigned>(BSYN) = 0;
      segD(IC<4>{}, IC<6>{});
      asm volatile("s_barrier" ::: "memory");
      segD(IC<5>{}, IC<6 + UPS>{});
      asm volatile("s_waitcnt vmcnt(16)\n\ts_barrier" ::: "memory");
      STAMP(19);
      segD(IC<6>{}, IC<6 + 2 * UPS>{});
      asm volatile("s_waitcnt vmcnt(16) lgkmcnt(0)\n\ts_barrier" ::: "memory");
      segW(IC<0>{}, IC<6 + 3 * UPS>{});
      asm volatile("s_waitcnt vmcnt(16)\n\ts_barrier" ::: "memory");
      STAMP(2);
      segW(IC<2>{}, IC<6 + 4 * UPS>{});
      asm volatile("s_waitcnt vmcnt(16)\n\ts_barrier" ::: "memory");
      STAMP(21);
      segW(IC<4>{}, IC<6 + 5 * UPS>{});
      VMW(16); readW(W, 0, 0); readW(W, 1, 16384);
      stepH(IC<6 + 6 * UPS>{});
      LGK0();
      dmaW(6, 0, 65536); dmaW(6, 1, 0); dmaW(6, 2, 0); dmaW(6, 3, 0);
      unsigned vf = (wq * 8 + (lane >> 3)) * 128, junk = 0; OPQ(vf);
      if (MTCH) junk = ld32(p.w2, lane < 56 ? lane * 128 : OOB, ((idx - 4) * 4 + wq) * 7168, 0);
      SETM0(km0);
      SB();
      sfor<7 + 6 * UPS, 44>([&](auto N) { stepH(N); });
      auto put = [&](unsigned (&t)[2][16], v4u (&f)[2][4], bool full) {
        kputA(t[0]); if (full) kputB(t[0]); frags(f[0]);
        kputA(t[1]); if (full) kputB(t[1]); frags(f[1]);
      };
      __builtin_amdgcn_s_setprio(BPRIO);
      v2u xf;
      ALD_B64S(xf, vf, p.ws, WS_FX);
      STAMP(5);
      put(tL, Bf, true); put(t0, Af, true);
      asm volatile("s_waitcnt vmcnt(0)" : "+v"(xf) :: "memory");
      OPQ(junk);
      await64(xf, p.ws, vf, WS_FX, tag);
      unsigned vu = (t8 >> 3) * XNS + (t8 & 7) * 32; OPQ(vu);
      const v4u ua = ld128(p.ws, vu, WS_XN + cp * 256, 16), ub = ld128(p.ws, vu + 16, WS_XN + cp * 256, 16);
      const unsigned dskp = ld32(p.Dsk, 0, cp * 4, 0);
      SB();
      kmm(0); put(t1, Af, true); readW(W, 6, 0);
      kmm(1); put(t2, Af, false); readW(W, 2, 32768);
      *lp<f2>(YSC + ch * 8) = f2{ysc, 0.f};
      kmm(2);
      {
        unsigned pwa = km0 + lane * 16; OPQ(pwa);
        *lp<f4>(pwa) = nh ? kc[0] : kc[1]; *lp<f4>(pwa + 1024) = kc[2];
        kc[0] = nh ? kc[1] : kc[0]; OPQ(kc[0]);
      }
      readW(W, 3, 49152);
      STAMP(6);
      bsync(1);
      {
        unsigned pra = (wq ^ 1) * KSCR + lane * 16; OPQ(pra);
        const f4 a = kc[0] + *lp<f4>(pra), b = kc[2] + *lp<f4>(pra + 1024);
        const unsigned h0 = pkh(a.xy), h1 = pkh(a.zw), h2_ = pkh(b.xy), h3 = pkh(b.zw);
        *lp<unsigned>(tw[0]) = h0; *lp<unsigned>(tw[1]) = h1;
        *lp<u16>(tw[2]) = (u16)h2_; *lp<u16>(tw[2] + 32) = (u16)(h2_ >> 16); *lp<u16>(tw[2] + 64) = (u16)h3; *lp<u16>(tw[2] + 96) = (u16)(h3 >> 16);
        *lp<v2u>(UARR + wq * UCP + min(lane, 55u) * 8) = v2u{0, 0};
        *lp<unsigned>(UARR + (lane & 1) * 2 * UCP + 448) = 0;
        *lp<unsigned>(BSY2) = 0;
        readW(W, 4, 65536);
        STAMP(7);
        bsync(2);
        unsigned tv[2][9];
#pragma unroll
        for (int c = 0; c < 2; ++c)
#pragma unroll
          for (int e = 0; e < 9; ++e) tv[c][e] = *lp<u16>(tr[e] + c * 1536);
        *lp<unsigned>(q4 * KCP + 4096 + l15 * 4) = 0;
        unsigned kb = t8 * 16; OPQ(kb);
#pragma unroll
        for (int c = 0; c < 2; ++c) {
          unsigned r0 = tv[c][0] | tv[c][1] << 16, r1 = tv[c][2] | tv[c][3] << 16, r2 = tv[c][4] | tv[c][5] << 16, r3 = tv[c][6] | tv[c][7] << 16;
          OPQ(r0); OPQ(r1); OPQ(r2); OPQ(r3);
          *lp<v4u>(kb + (2 * c + 1) * KCP) = v4u{r0, r1, r2, r3};
          *lp<v4u>(kb + 2 * c * KCP) = v4u{__builtin_amdgcn_alignbit(r1, r0, 16), __builtin_amdgcn_alignbit(r2, r1, 16), __builtin_amdgcn_alignbit(r3, r2, 16),
                                           __builtin_amdgcn_alignbit(t8 == 255 ? 0u : tv[c][8], r3, 16)};
        }
      }
      STAMP(20);
      {
        unsigned uw = UARR + 448 + t8 * 16; OPQ(uw);
#pragma unroll
        for (int c = 0; c < 2; ++c) {
          const unsigned sel = c ? 0x07060302u : 0x05040100u;
          const unsigned a0 = __builtin_amdgcn_perm(ua.y, ua.x, sel), a1 = __builtin_amdgcn_perm(ua.w, ua.z, sel);
          const unsigned a2 = __builtin_amdgcn_perm(ub.y, ub.x, sel), a3 = __builtin_amdgcn_perm(ub.w, ub.z, sel);
          *lp<v4u>(uw + (2 * c + 1) * UCP) = v4u{a0, a1, a2, a3};
          *lp<unsigned>(uw + 2 * c * UCP + 4) = a0; *lp<v2u>(uw + 2 * c * UCP + 8) = v2u{a1, a2}; *lp<unsigned>(uw + 2 * c * UCP + 16) = a3;
        }
      }
      unsigned sh0 = WS_H + (2 * tb * 112 + 14 * w) * 1024, vl = lane * 16;
      unsigned fa = (ch * 2 + (l15 & 1)) * KCP + 2 * (2044 - 1792 - 96 - 128 * nh - 4 * (l15 >> 1) + 8 * q4), fb = fa + 8;
      unsigned ga = UARR + (ch * 2 + (l15 & 1)) * UCP + 2 * (224 - 96 - 128 * nh + 32 * (l15 >> 1) + 8 * q4);
      OPQS(sh0); OPQ(vl); OPQ(fa); OPQ(fb); OPQ(ga);
      auto loadH = [&](int n) { const int half = n / 14, k = n % 14; H[n] = ld128(p.ws, vl + (k & 3) * 1024, sh0 + half * 112 * 1024 + (k >> 2) * 4096, 16); };
      f4 ya[8];
#pragma unroll
      for (int i = 0; i < 8; ++i) ya[i] = f4{0.f, 0.f, 0.f, 0.f};
      readW(W, 5, 81920);
      STAMP(17);
      BAR();
      STAMP(9);
      unsigned vq = hf * 4096 + min(((14 * w) >> 2) + ((lane >> 2) & 7), (14 * w + 13) >> 2) * 128; OPQ(vq);
      v2u hfl;
      conv_all(fa, fb, ga, ya, [&](auto M) {
        if constexpr (M.v == 112) hfl = ld64(p.ws, vq, WS_FH + tb * 8192, 16);
      });
      STAMP(10);
      unsigned y0a = YBUF + wq * 8192 + 4 * (32 * (l15 >> 1) + ((2 * (l15 & 1) + 8 * q4) ^ (4 * (l15 >> 1)))), y0b = y0a ^ 16;
      OPQ(y0a); OPQ(y0b);
#pragma unroll
      for (int i = 0; i < 8; ++i) { *lp<f2>(y0a + i * 1024) = ya[i].xy; *lp<f2>(y0b + i * 1024) = ya[i].zw; SB(); }
      unsigned yr = YBUF + wq * 2048 + ((lane ^ (lane >> 3)) << 4), ur = UARR + UCP + 448 + wq * 1024 + lane * 8, vs = WS_S + cp * SST + wq * 4096 + lane * 16;
      OPQ(yr); OPQ(ur); OPQ(vs);
      if (NHE) await64(hfl, p.ws, vq, WS_FH + tb * 8192, tag);
      sfor<0, NHE / 2>([&](auto N) { loadH(N.v); });
      bsync(3);
      STAMP(11);
      {
        const f4 sc = *lp<f4>(YSC);
        f4 yv[2][4];
        v2u uv[2][2];
#pragma unroll
        for (int t = 0; t < 2; ++t) {
#pragma unroll
          for (int k = 0; k < 4; ++k) yv[t][k] = *lp<f4>(yr + t * 1024 + k * 8192);
          uv[t][0] = *lp<v2u>(ur + t * 512); SB(); uv[t][1] = *lp<v2u>(ur + t * 512 + 2 * UCP); SB();
        }
        sfor<NHE / 2, NHE>([&](auto N) { loadH(N.v); });
        SB();
        const float D0 = bflo(dskp), D1 = bfhi(dskp);
#pragma unroll
        for (int t = 0; t < 2; ++t) {
          float y0[4], y1[4];
#pragma unroll
          for (int i = 0; i < 4; ++i) { y0[i] = yv[t][0][i] + yv[t][1][i]; y1[i] = yv[t][2][i] + yv[t][3][i]; OPQ(y0[i]); OPQ(y1[i]); y0[i] *= sc.x; y1[i] *= sc.z; OPQ(y0[i]); OPQ(y1[i]); }
          const h2 a0 = __builtin_bit_cast(h2, uv[t][0].x + 0u), a1 = __builtin_bit_cast(h2, uv[t][0].y + 0u);
          const h2 b0 = __builtin_bit_cast(h2, uv[t][1].x + 0u), b1 = __builtin_bit_cast(h2, uv[t][1].y + 0u);
          const v4u sv = {pkbf(f2{__builtin_fmaf((float)a0.x, D0, y0[0]), __builtin_fmaf((float)b0.x, D1, y1[0])}),
                          pkbf(f2{__builtin_fmaf((float)a0.y, D0, y0[1]), __builtin_fmaf((float)b0.y, D1, y1[1])}),
                          pkbf(f2{__builtin_fmaf((float)a1.x, D0, y0[2]), __builtin_fmaf((float)b1.x, D1, y1[2])}),
                          pkbf(f2{__builtin_fmaf((float)a1.y, D0, y0[3]), __builtin_fmaf((float)b1.y, D1, y1[3])})};
          st128(v4u{sv.x, (unsigned)tag, sv.y, (unsigned)tag}, p.ws, vs + t * 2048, 0, 16);
          st128(v4u{sv.z, (unsigned)tag, sv.w, (unsigned)tag}, p.ws, vs + t * 2048 + 1024, 0, 16);
        }
      }
      STAMP(12);
      if (!NHE) await64(hfl, p.ws, vq, WS_FH + tb * 8192, tag);
      LGK0();
      if (lane == 0) __hip_atomic_fetch_add(lp<unsigned>(BSY2), 1u, __ATOMIC_RELAXED, __HIP_MEMORY_SCOPE_WORKGROUP);
      __builtin_amdgcn_s_setprio(0);
      sfor<NHE, NHB>([&](auto N) { loadH(N.v); });
      bwait(BSY2, 4);
      STAMP(18);
      tail(IC<1>{}, W, H, IC<NHB>{}, loadH, tag);
    }
  }
  asm volatile("s_waitcnt vmcnt(0) lgkmcnt(0)" : "+s"(ert0), "+s"(ert) :: "memory");
  {
    u64* dbg = (u64*)((char*)(((u64)(unsigned)p.ws.y << 32) | (unsigned)p.ws.x) + WS_DBG) + (blockIdx.x * 8 + w) * 2;
    if (lane == 0) { dbg[0] = ert0; dbg[1] = ert; }
  }
}

__global__ void tiny_kernel(char* ws) {
  const u64* d = (const u64*)(ws + WS_DBG);
  u64 mn = ~0ull, sum = 0, mx = 0;
  for (int i = threadIdx.x; i < 2048; i += 64) { mn = d[2 * i] < mn ? d[2 * i] : mn;
    const int role = i < 256 ? 2 : (i >> 2) & 1; if (role == ENCR) { sum += d[2 * i + 1]; mx = d[2 * i + 1] > mx ? d[2 * i + 1] : mx; } }
  for (int o = 32; o; o >>= 1) { u64 a = __shfl_xor(mn, o), b = __shfl_xor(sum, o), c = __shfl_xor(mx, o); mn = a < mn ? a : mn; sum += b; mx = c > mx ? c : mx; }
  u64 val = ENCR == 2 ? mx - mn : sum / 896 - mn; if (val > 5000) val = 5000;
  const u64 target = mn + 6000 + 4 * val;
  for (int n = 0; n < 200000; ++n) { if (__builtin_amdgcn_s_memrealtime() >= target) break; __builtin_amdgcn_s_sleep(1); }
}
extern "C" void kernel_launch(void* const* d_in, const int* in_sizes, int n_in, void* d_out, int out_size, void* d_ws, size_t ws_size,
                              hipStream_t stream) {
  const unsigned FL = 0x00020000u;
  HostParams h = {{{d_in[1], DM * 512, FL}, {d_in[2], DM * 512, FL}, {d_in[3], DM * 512, FL}, {d_in[4], DM * 512, FL},
                   {d_ws, WS_END, FL}, {d_in[0], LT * DM * 2, FL}, {d_in[8], DM * FF * 2 + 4096, FL}, {d_in[9], FF * 2, FL},
                   {d_in[5], DM * 2, FL}, {d_in[10], FF * DM * 2, FL}, {d_in[6], DM * 2, FL}, {d_in[11], DM * 2, FL},
                   {d_out, LT * DM * 2, FL}}};
  static_assert(sizeof(HostParams) == sizeof(Params), "descriptor block layout");
  Params p;
  __builtin_memcpy(&p, &h, sizeof(p));
  s4d_mlp_gate_kernel<<<256, 512, 0, stream>>>(p);
  tiny_kernel<<<1, 64, 0, stream>>>((char*)d_ws);
}
```

```cpp
#include <hip/hip_runtime.h>
#include <hip/hip_bf16.h>

typedef unsigned short u16;
typedef unsigned long long u64;
typedef int v4i __attribute__((ext_vector_type(4)));
typedef unsigned v4u __attribute__((ext_vector_type(4)));
typedef unsigned v2u __attribute__((ext_vector_type(2)));
typedef float f2 __attribute__((ext_vector_type(2)));
typedef float f4 __attribute__((ext_vector_type(4)));
typedef float f16v __attribute__((ext_vector_type(16)));
typedef short s4 __attribute__((ext_vector_type(4)));
typedef unsigned short us2 __attribute__((ext_vector_type(2)));
typedef __bf16 b8 __attribute__((ext_vector_type(8)));
typedef __bf16 b2 __attribute__((ext_vector_type(2)));
typedef _Float16 h8 __attribute__((ext_vector_type(8)));
typedef _Float16 h2 __attribute__((ext_vector_type(2)));
#define LAS __attribute__((address_space(3)))
#define DEV __device__ __forceinline__
#define SB() __builtin_amdgcn_sched_barrier(0)
#define VMW(n) asm volatile("s_waitcnt vmcnt(" #n ")" ::: "memory")
#define LGK0() asm volatile("s_waitcnt lgkmcnt(0)" ::: "memory")
#define BAR() asm volatile("s_waitcnt lgkmcnt(0)\n\ts_barrier" ::: "memory")
#define OPQ(x) asm volatile("" : "+v"(x))
#define OPQS(x) asm volatile("" : "+s"(x))

#define STAMPS 0
#define STAMP(i) do {} while (0)

constexpr int LT = 2048, DM = 448, FF = 1792;
constexpr unsigned WS_E = 0, WS_FX = 4096, WS_ST = 32768, WS_FH = 65536, WS_FS = WS_FH + 64 * 4096, WS_XN = WS_FS + 56 * 4096,
                   WS_S = WS_XN + 224 * 8192, WS_H = WS_S + 224 * 8192, WS_END = WS_H + 64 * 112 * 1024, WS_DBG = 16u << 20;
constexpr unsigned KCP = 4224, KROW = 288, KSCR = 16 * KROW, TTAB = 28672, UARR = 32768, UCP = 4640, CSX = 24576, YSC = 31744, YBUF = 65536, SBUF = 131072;
constexpr unsigned slot_of(int i) { return 32768u + ((i + 2) & 3) * 32768u; }
constexpr unsigned wslot_of(int i) { return 65536u + 32768u * (i % 3); }
#define HPF 0
#define HST 44
#define KEARLY 0
constexpr unsigned CODE_PF_BYTES = 19456;
constexpr unsigned SPIN_CAP = 1u << 14;
constexpr unsigned OOB = 0x70000000u;

struct Buf { const void* p; unsigned bytes, flags; };
struct HostParams { Buf b[13]; };
struct Params { v4i lA, Ai, Cr, Ci, ws, x, w1, b1, ldt, w2, Dsk, b2, out; };

__device__ v4u ld128(v4i rs, unsigned vo, unsigned so, int aux) __asm("llvm.amdgcn.raw.buffer.load.v4i32");
__device__ v2u ld64(v4i rs, unsigned vo, unsigned so, int aux) __asm("llvm.amdgcn.raw.buffer.load.v2i32");
__device__ unsigned ld32(v4i rs, unsigned vo, unsigned so, int aux) __asm("llvm.amdgcn.raw.buffer.load.i32");
__device__ void st128(v4u v, v4i rs, unsigned vo, unsigned so, int aux) __asm("llvm.amdgcn.raw.buffer.store.v4i32");
__device__ void st64(v2u v, v4i rs, unsigned vo, unsigned so, int aux) __asm("llvm.amdgcn.raw.buffer.store.v2i32");

DEV float asf(unsigned u) { return __builtin_bit_cast(float, u); }
DEV unsigned asu(float f) { return __builtin_bit_cast(unsigned, f); }
DEV float bflo(unsigned u) { return asf(u << 16); }
DEV float bfhi(unsigned u) { return asf(u & 0xffff0000u); }
DEV f2 bf2(unsigned u) { return f2{bflo(u), bfhi(u)}; }
DEV unsigned pkbf(f2 v) { return __builtin_bit_cast(unsigned, __builtin_convertvector(v, b2)); }
DEV unsigned pkh(f2 v) { return __builtin_bit_cast(unsigned, __builtin_convertvector(v, h2)); }
DEV float rbf(float a) { return bflo(pkbf(f2{a, a})); }
DEV f2 h2f(unsigned u) { h2 h = __builtin_bit_cast(h2, u); return f2{(float)h.x, (float)h.y}; }
DEV f2 fma2(f2 a, f2 b, f2 c) { return __builtin_elementwise_fma(a, b, c); }
DEV float dot2(unsigned a, unsigned b, float s) { return __builtin_amdgcn_fdot2_f32_bf16(__builtin_bit_cast(b2, a), __builtin_bit_cast(b2, b), s, false); }
template <typename T> DEV LAS T* lp(unsigned a) { return (LAS T*)(size_t)a; }
#define ALD_U16(d, vo, rs, so) asm volatile("buffer_load_ushort %0, %1, %2, %3 offen" : "=v"(d) : "v"(vo), "s"(rs), "s"(so) : "memory")
#define ALD_B64(d, vo, rs, so) asm volatile("buffer_load_dwordx2 %0, %1, %2, %3 offen" : "=v"(d) : "v"(vo), "s"(rs), "s"(so) : "memory")
#define ALD_B64S(d, vo, rs, so) asm volatile("buffer_load_dwordx2 %0, %1, %2, %3 offen sc1" : "=v"(d) : "v"(vo), "s"(rs), "s"(so) : "memory")
#define ALD_B128S(d, vo, rs, so) asm volatile("buffer_load_dwordx4 %0, %1, %2, %3 offen sc1" : "=v"(d) : "v"(vo), "s"(rs), "s"(so) : "memory")
DEV v4u trfrag(unsigned a) {
  s4 lo = __builtin_amdgcn_ds_read_tr16_b64_v4i16(lp<s4>(a)), hi = __builtin_amdgcn_ds_read_tr16_b64_v4i16(lp<s4>(a + 1024));
  v2u l = __builtin_bit_cast(v2u, lo), h = __builtin_bit_cast(v2u, hi);
  return v4u{l.x, l.y, h.x, h.y};
}
DEV f16v mfma32(v4u a, v4u b, f16v c) { return __builtin_amdgcn_mfma_f32_32x32x16_bf16(__builtin_bit_cast(b8, a), __builtin_bit_cast(b8, b), c, 0, 0, 0); }
DEV f4 mfma16h(v4u a, v4u b, f4 c) { return __builtin_amdgcn_mfma_f32_16x16x32_f16(__builtin_bit_cast(h8, a), __builtin_bit_cast(h8, b), c, 0, 0, 0); }
template <int C> DEV float dppf(float v) { return asf(__builtin_amdgcn_update_dpp(0, asu(v), C, 0xf, 0xf, true)); }
DEV float sum8(float v) { v += dppf<0xB1>(v); v += dppf<0x4E>(v); v += dppf<0x141>(v); return v; }
template <int C, int RM> DEV unsigned dppu(unsigned v) { return __builtin_amdgcn_update_dpp(0, v, C, RM, 0xf, true); }
DEV unsigned wave_maxu(unsigned v) {
  v = max(v, dppu<0xB1, 0xf>(v)); v = max(v, dppu<0x4E, 0xf>(v)); v = max(v, dppu<0x141, 0xf>(v)); v = max(v, dppu<0x140, 0xf>(v));
  v = max(v, dppu<0x142, 0xa>(v)); v = max(v, dppu<0x143, 0xc>(v));
  return __builtin_amdgcn_readlane(v, 63);
}
DEV u64 as64(v2u v) { return __builtin_bit_cast(u64, v); }
DEV void await64(v2u f, v4i rs, unsigned vo, unsigned so, u64 tag) {
  if (__builtin_expect(__builtin_amdgcn_ballot_w64(as64(f) != tag) != 0, 0)) {
#pragma unroll 1
    for (unsigned n = 0; n < SPIN_CAP; ++n) {
      asm volatile("buffer_load_dwordx2 %0, %1, %2, %3 offen sc1\n\ts_waitcnt vmcnt(0)" : "=&v"(f) : "v"(vo), "s"(rs), "s"(so) : "memory");
      if (__builtin_amdgcn_ballot_w64(as64(f) != tag) == 0) break;
      __builtin_amdgcn_s_sleep(1);
    }
  }
}
struct cm { f2 w, r; };
DEV cm mkcm(f2 w) { return cm{w, f2{-w.y, w.x}}; }
DEV f2 cmul(f2 a, cm m) { return fma2(a.xx, m.w, a.yy * m.r); }
template <int N> DEV void kput(const unsigned (&v)[16]) {
  asm volatile("ds_write_addtid_b32 %0 offset:0\n\tds_write_addtid_b32 %1 offset:288\n\tds_write_addtid_b32 %2 offset:576\n\tds_write_addtid_b32 %3 offset:864\n\t"
               "ds_write_addtid_b32 %4 offset:1152\n\tds_write_addtid_b32 %5 offset:1440\n\tds_write_addtid_b32 %6 offset:1728\n\tds_write_addtid_b32 %7 offset:2016\n\t"
               "ds_write_addtid_b32 %8 offset:2304"
               :: "v"(v[0]), "v"(v[1]), "v"(v[2]), "v"(v[3]), "v"(v[4]), "v"(v[5]), "v"(v[6]), "v"(v[7]), "v"(v[8]) : "memory");
  if (N > 9)
    asm volatile("ds_write_addtid_b32 %0 offset:2592\n\tds_write_addtid_b32 %1 offset:2880\n\tds_write_addtid_b32 %2 offset:3168\n\tds_write_addtid_b32 %3 offset:3456\n\t"
                 "ds_write_addtid_b32 %4 offset:3744\n\tds_write_addtid_b32 %5 offset:4032\n\tds_write_addtid_b32 %6 offset:4320"
                 :: "v"(v[9]), "v"(v[10]), "v"(v[11]), "v"(v[12]), "v"(v[13]), "v"(v[14]), "v"(v[15]) : "memory");
}
static_assert(KROW == 288, "offsets above");
#define SETM0(x) asm volatile("s_mov_b32 m0, %0\n\ts_nop 0" :: "s"(x) : "memory")
#define DMA(vo, rs, so, imm) asm volatile("buffer_load_dwordx4 %0, %1, %2 offen offset:" #imm " lds" :: "v"(vo), "s"(rs), "s"(so) : "memory")

template <int TH> struct ConvPlan {
  static constexpr int PI[18] = {0, 0, 0, 0, 1, 1, 1, TH ? 1 : 2, 2, 2, TH ? 2 : 3, 3, 3, 4, 4, 5, TH ? 5 : 6, TH ? 6 : 7};
  static constexpr int PE0[18] = {0, 3, 4, 7, 3, 2, 6, 2, 1, 5, 0, 1, 4, 0, 3, 2, 1, 0};
  static constexpr int PE1[18] = {1, 2, 5, 6, 5, 1, 0, 4, 4, 0, 3, 3, 2, 2, 1, 1, 0, 0};
  static constexpr int pe(int m) { return TH ? PE1[m] : PE0[m]; }
  static constexpr int acc(int m) { const int t = PI[m] + pe(m); return TH ? (t == 1 ? 0 : t == 2 ? 1 : t == 5 ? 2 : 3) : (t == 0 ? 0 : t == 3 ? 1 : t == 4 ? 2 : 3); }
  static constexpr bool newF(int m) { return m == 0 || PI[m] != PI[m - 1]; }
  static constexpr bool newG(int m) { for (int k = 0; k < m; ++k) if (pe(k) == pe(m)) return false; return true; }
};
template <int V> struct IC { static constexpr int v = V; };
template <int B, int E, typename FN> DEV void sfor(FN&& f) { if constexpr (B < E) { f(IC<B>{}); sfor<B + 1, E>(f); } }
template <int TH, typename FN> DEV void conv_all(unsigned fa, unsigned fb, unsigned ga, f4 (&acc)[4], FN&& hook) {
  using P = ConvPlan<TH>;
  constexpr int LA = 6;
  v4u Fv[2][8], Gv[2][8];
  auto fetch = [&](auto M) {
    constexpr int jj = M.v / 18, pm = M.v % 18, i = P::PI[pm], e = P::pe(pm);
    if constexpr (P::newF(pm)) {
      v2u a = *lp<v2u>(fa + 512 * (7 - i) + 64 * (3 - jj)), b = *lp<v2u>(fb + 512 * (7 - i) + 64 * (3 - jj));
      Fv[jj & 1][i] = v4u{a.x, a.y, b.x, b.y};
    }
    if constexpr (P::newG(pm)) Gv[jj & 1][e] = *lp<v4u>(ga + 512 * e + 64 * (3 - jj));
  };
  sfor<0, LA>([&](auto M) { fetch(M); SB(); });
  sfor<0, 72>([&](auto M) {
    constexpr int jj = M.v / 18, pm = M.v % 18, i = P::PI[pm], e = P::pe(pm), t = P::acc(pm);
    acc[t] = mfma16h(Fv[jj & 1][i], Gv[jj & 1][e], acc[t]);
    SB();
    if constexpr (M.v + LA < 72) fetch(IC<M.v + LA>{});
    hook(M);
    SB();
  });
}

__global__ __launch_bounds__(512, 2) void s4d_mlp_gate_kernel(Params p) {
  __shared__ __attribute__((aligned(16))) char smem[163840];
  asm volatile("" :: "v"((LAS char*)smem) : "memory");
  const unsigned tid = threadIdx.x, lane = tid & 63;
  const unsigned w = __builtin_amdgcn_readfirstlane(tid >> 6);
  const unsigned xcd = blockIdx.x & 7, idx = blockIdx.x >> 3;
  {
    u64 pc; asm volatile("s_getpc_b64 %0" : "=s"(pc));
    const v4i rC = {(int)(unsigned)pc, (int)(unsigned)(pc >> 32), (int)CODE_PF_BYTES, 0x00020000};
    asm volatile("s_mov_b32 m0, %0\n\ts_nop 0\n\tbuffer_load_dword %1, %2, %3 offen lds" :: "s"(slot_of(3) + w * 4096),
                 "v"(lane < 4 ? lane * 128 : OOB), "s"(rC), "s"(((blockIdx.x & 56) + w) * 512) : "memory");
    SB();
  }
  if (idx < 4) {
    const unsigned hid = xcd * 4 + idx, tok0 = hid * 64 + w * 8, r8 = lane >> 3, part = lane & 7;
    v2u Ev;
    unsigned vo = r8 * 896 + part * 16, so = tok0 * 896, zero = 0;
    OPQ(vo); OPQS(so); OPQ(zero); SB();
    v4u xr[7];
#pragma unroll
    for (int k = 0; k < 7; ++k) xr[k] = ld128(p.x, vo + k * 128, so, 0);
    ALD_B64S(Ev, zero, p.ws, 0u);
    unsigned vt = lane < 56 ? lane * 128 : OOB, st0 = (w * 7 * 32 + idx * 8) * 896;
    OPQ(vt); OPQS(st0);
    SETM0(65536 + w * 256);
    auto touch = [&](v4i rs, unsigned vo, unsigned so) { asm volatile("buffer_load_dword %0, %1, %2 offen lds" :: "v"(vo), "s"(rs), "s"(so) : "memory"); };
    float s0 = 0.f, s1 = 0.f, q0 = 0.f, q1 = 0.f;
#pragma unroll
    for (int k = 0; k < 7; ++k) {
      s0 = dot2(xr[k].x, 0x3f803f80u, s0); q0 = dot2(xr[k].x, xr[k].x, q0); s1 = dot2(xr[k].y, 0x3f803f80u, s1); q1 = dot2(xr[k].y, xr[k].y, q1);
      s0 = dot2(xr[k].z, 0x3f803f80u, s0); q0 = dot2(xr[k].z, xr[k].z, q0); s1 = dot2(xr[k].w, 0x3f803f80u, s1); q1 = dot2(xr[k].w, xr[k].w, q1);
    }
    OPQ(s0); OPQ(s1); OPQ(q0); OPQ(q1);
    touch(p.w2, vt, st0); touch(p.w2, vt, st0 + 1 * 32 * 896);
    STAMP(1);
    const float mean = sum8(s0 + s1) * (1.0f / DM);
    const float var = fmaxf(__builtin_fmaf(-mean, mean, sum8(q0 + q1) * (1.0f / DM)), 0.f);
    const float mb = rbf(mean);
    const float sd1 = rbf(__builtin_amdgcn_sqrtf(rbf(rbf(var) + 1e-5f)));
    const float rs1 = __builtin_amdgcn_rcpf(sd1);
    const float mb2 = rbf((mean - mb) * rs1);
    const float sd2 = rbf(__builtin_amdgcn_sqrtf(rbf(rbf(var * rs1 * rs1) + 1e-5f)));
    asm volatile("s_waitcnt vmcnt(2)" : "+v"(Ev) :: "memory");
    const u64 tag = (((u64)(unsigned)__builtin_amdgcn_readfirstlane(Ev.y) << 32) | (unsigned)__builtin_amdgcn_readfirstlane(Ev.x)) + 1;
    const v2u tg2 = {(unsigned)tag, (unsigned)(tag >> 32)};
    st128(v4u{asu(-__builtin_fmaf(mb2, sd1, mb)), tg2.x, asu(sd1 * sd2), tg2.x}, p.ws, part == 0 ? WS_ST + (tok0 + r8) * 16 : OOB, 0, 16);
    STAMP(2);
    {
      unsigned la = part * 1024 + ((((2 * w + (r8 >> 2)) ^ part) << 4) | ((r8 & 3) << 2));
      OPQ(la);
      const f2 rs2 = {rs1, rs1}, off2 = {-mb * rs1, -mb * rs1};
#pragma unroll
      for (int k = 0; k < 7; ++k)
#pragma unroll
        for (int i = 0; i < 4; ++i) *lp<unsigned>(la + k * 8192 + i * 256) = pkh(fma2(bf2(xr[k][i]), rs2, off2));
    }
    BAR();
    STAMP(3);
    {
      v4u v[7];
#pragma unroll
      for (int k = 0; k < 7; ++k) v[k] = *lp<v4u>(tid * 16 + k * 8192);
      const unsigned go = WS_XN + hid * 256 + (tid >> 4) * 8192 + (((tid & 15) ^ (w & 7)) << 4);
#pragma unroll
      for (int k = 0; k < 7; ++k) st128(v[k], p.ws, go + k * 32 * 8192, 0, 16);
    }
    touch(p.w2, vt, st0 + 2 * 32 * 896);
    VMW(1);
    BAR();
    STAMP(4);
    if (tid == 0) st64(tg2, p.ws, 0, WS_FX + hid * 128, 16);
    touch(p.w2, vt, st0 + 3 * 32 * 896); touch(p.w2, vt, st0 + 4 * 32 * 896); touch(p.w2, vt, st0 + 5 * 32 * 896); touch(p.w2, vt, st0 + 6 * 32 * 896);
    STAMP(6);
    STAMP(7);
    if (hid == 0) {
      const unsigned vo2 = tid < 224 ? WS_FH + ((tid / 14) * 4 * 4096) + (tid % 14) * 256 : WS_FX + (tid & 31) * 128;
      if (w < 4) await64(v2u{tg2.x + 1, tg2.y}, p.ws, vo2, 0u, tag);
      BAR();
      if (tid == 0) st64(tg2, p.ws, 0, WS_E, 16);
    }
    STAMP(5);
  } else {
    const unsigned ma = idx & 3, mb_ = (idx >> 2) - 1;
    const unsigned tt = 4 * (xcd >> 1) + ma, sl = 7 * (xcd & 1) + mb_, tb = 4 * xcd + ma, cb = mb_, cp = 28 * xcd + idx - 4;
        const bool rot = (ma + mb_) & 1;
    const unsigned tg = w & 3, hg = w >> 2, hf = lane >> 5, l31 = lane & 31, l15 = lane & 15, q4 = lane >> 4;
    unsigned vx0 = (lane >> 3) * 896 + (((lane & 7) ^ q4) << 4);
    unsigned vx1 = (vx0 ^ 64) + 8 * 896 - 1024;
    unsigned vw0 = q4 * 3584 + ((l15 ^ (q4 << 2)) << 4) + 2048;
    unsigned vw1 = vw0 + 4 * 3584 - 1024;
    unsigned sxA = (tt * 128 + w * 16) * 896 + (rot ? 4 * 128 : 0), swA = w * 8 * 3584 + sl * 256 + (rot ? 4 * 64 * 3584 : 0);
    unsigned vp = tid * 2, sp = cp * 1024, vc = lane * 8, sc = sp + hg * 512, vb = l31 * 2, sb1 = sl * 256 + hg * 128, zero = 0, sdt = cp * 4 + hg * 2;
    unsigned m0w = w * 4096; OPQS(m0w);
    OPQ(vx0); OPQ(vx1); OPQ(vw0); OPQ(vw1); OPQ(vp); OPQ(vc); OPQ(vb); OPQ(zero);
    OPQS(sxA); OPQS(swA); OPQS(sp); OPQS(sc); OPQS(sb1); OPQS(sdt);
    SB();
    unsigned plA, pAi, pCr, pCi, pb1a, pb1b, pdt;
    v2u Ev, mCr, mCi;
    ALD_U16(pdt, zero, p.ldt, sdt);
    ALD_U16(plA, vp, p.lA, sp);
    ALD_U16(pAi, vp, p.Ai, sp);
    ALD_U16(pCr, vp, p.Cr, sp);
    ALD_U16(pCi, vp, p.Ci, sp);
    ALD_B64(mCr, vc, p.Cr, sc);
    ALD_B64(mCi, vc, p.Ci, sc);
    v4i rW1 = p.w1;
    { const u64 b = (((u64)(unsigned)rW1.y << 32) | (unsigned)rW1.x) - 4096; rW1.x = (int)(unsigned)b; rW1.y = (int)(unsigned)(b >> 32); }
    unsigned sxB, swB;
    auto dmaD = [&](int i, int j) {
      const unsigned sx = (i < 3 ? sxA : sxB) + i * 128, sw = (i < 3 ? swA : swB) + i * (64 * 3584);
      if (j == 0) DMA(vx0, p.x, sx, 0); else if (j == 1) DMA(vx1, p.x, sx, 1024);
      else if (j == 2) DMA(vw0, rW1, sw, 2048); else DMA(vw1, rW1, sw, 3072);
    };
    SETM0(m0w + slot_of(0));
    asm volatile("s_barrier" ::: "memory");
    dmaD(0, 0); dmaD(0, 1); dmaD(0, 2); dmaD(0, 3);
    ALD_B64S(Ev, zero, p.ws, 0u);
    ALD_U16(pb1a, vb, p.b1, sb1);
    asm volatile("buffer_load_ushort %0, %1, %2, %3 offen offset:64" : "=v"(pb1b) : "v"(vb), "s"(p.b1), "s"(sb1) : "memory");
    SB();
    unsigned aB[2][4], aA[2][2][4];
    {
      const unsigned qa = l15 >> 2, pp = lane & 3, blk = q4 & 1;
      const unsigned b0 = 32768 + (2 * tg + (l31 >> 4)) * 4096 + l15 * 128, fx = (hf ^ ((l31 >> 1) & 7)) << 4;
      const unsigned a0 = 32768 + 2048 + hf * 4096 + qa * 256 + (((8 * hg + 2 * blk + (pp >> 1)) ^ (qa << 2)) << 4) + 8 * (pp & 1);
#pragma unroll
      for (int i = 0; i < 4; ++i) {
        const unsigned ks = (i + tg) & 3;
        aB[0][i] = b0 + (fx ^ (ks << 5)); aB[1][i] = aB[0][i] + 65536;
        aA[0][0][i] = a0 + ks * 8192; aA[0][1][i] = aA[0][0][i] ^ 64; aA[1][0][i] = aA[0][0][i] + 65536; aA[1][1][i] = aA[0][1][i] + 65536;
        OPQ(aB[0][i]); OPQ(aB[1][i]); OPQ(aA[0][0][i]); OPQ(aA[0][1][i]); OPQ(aA[1][0][i]); OPQ(aA[1][1][i]);
        SB(); if (i == 0) SETM0(m0w + slot_of(1)); dmaD(1, i); SB();
      }
    }
    unsigned km0 = ((w + 7) & 7) * KSCR; OPQS(km0);
    unsigned kfa = km0 + l15 * KROW + q4 * 16;
    OPQ(kfa);
    sxB = sxA - (rot ? 7 * 128 : 0); swB = swA - (rot ? 7 * 64 * 3584 : 0); OPQS(sxB); OPQS(swB);
    unsigned vw2 = (lane >> 3) * 896 + (((lane & 7) ^ ((q4 & 1) << 2)) << 4), vst = l31 * 16, sst = WS_ST + (tt * 128 + tg * 32) * 16;
    unsigned sv0 = w * 7 * 32 * 896 + cb * 128;
    OPQ(vw2); OPQ(vst); OPQS(sst); OPQS(sv0);
    auto dmaW = [&](int i, int j) {
      const unsigned s = sv0 + i * (32 * 896);
      if (j == 0) { SETM0(m0w + wslot_of(i)); DMA(vw2, p.w2, s, 0); } else if (j == 1) DMA(vw2, p.w2, s + 6144, 1024);
      else if (j == 2) DMA(vw2, p.w2, s + 12288, 2048); else DMA(vw2, p.w2, s + 18432, 3072);
    };
    v4u W[7][4];
    unsigned aW[2][2];
    aW[0][0] = 65536 + w * 4096 + (4 * hf + (l15 >> 2)) * 128 + (((2 * (q4 & 1) + ((lane & 3) >> 1)) ^ (((l15 >> 3) & 1) << 2)) << 4) + 8 * (lane & 1);
    aW[0][1] = aW[0][0] ^ 64;
    OPQ(aW[0][0]); OPQ(aW[0][1]);
    auto readW1 = [&](int i, int f) { W[i][f] = trfrag(aW[wslot_of(i) == 131072][f & 1] + (wslot_of(i) == 98304 ? 32768 : 0) + (f >> 1) * 2048); };
    auto readW = [&](int i) { readW1(i, 0); readW1(i, 1); readW1(i, 2); readW1(i, 3); };
    SB();
    asm volatile("s_waitcnt vmcnt(11)" : "+v"(pdt), "+v"(plA), "+v"(pAi), "+v"(pCr), "+v"(pCi), "+v"(mCr), "+v"(mCi) :: "memory");
    STAMP(1);

    f4 kc[3];
    float ysc;
    v4u Bf[4], Af[4];
    auto frags = [&](v4u (&f)[4]) {
#pragma unroll
      for (int s = 0; s < 4; ++s) f[s] = *lp<v4u>(kfa + s * 64);
    };
    auto kmm = [&](int t) {
      kc[t] = f4{0.f, 0.f, 0.f, 0.f};
#pragma unroll
      for (int s = 0; s < 4; ++s) kc[t] = mfma16h(Af[s], Bf[s], kc[t]);
    };
    {
      constexpr float L2E = 1.4426950408889634f;
      const float xd = bflo(pdt), td = xd * L2E;
      const float ed = __builtin_amdgcn_exp2f(td), rd = __builtin_fmaf(xd, 1.925963033500011e-8f, __builtin_fmaf(xd, L2E, -td));
      const float dt = rbf(__builtin_fmaf(ed, rd * 0.6931471805599453f, ed));
      const float l2 = -rbf(__builtin_amdgcn_exp2f(bflo(plA) * L2E)) * dt * L2E;
      const float th = bflo(pAi) * dt * 0.15915494309189535f;
      auto cexp = [&](float s) {
        const float mg = __builtin_amdgcn_exp2f(l2 * s), f = __builtin_amdgcn_fractf(th * s);
        return f2{__builtin_amdgcn_cosf(f), __builtin_amdgcn_sinf(f)} * mg;
      };
      us2 mx = __builtin_elementwise_max(__builtin_elementwise_max(__builtin_bit_cast(us2, mCr.x & 0x7fff7fffu), __builtin_bit_cast(us2, mCr.y & 0x7fff7fffu)),
                                         __builtin_elementwise_max(__builtin_bit_cast(us2, mCi.x & 0x7fff7fffu), __builtin_bit_cast(us2, mCi.y & 0x7fff7fffu)));
      unsigned ew = wave_maxu(max((unsigned)mx.x, (unsigned)mx.y)) >> 7;
      ew = ew < 1u ? 1u : ew > 253u ? 253u : ew;
      ysc = 2.f * dt * asf(ew << 23);
      unsigned tL[16], t0[16], t1[16], t2[16];
      {
        f2 w8 = cexp(8.f); w8.y = -w8.y;
        const f2 w16 = cmul(w8, mkcm(w8));
        const cm m16 = mkcm(w16);
        f2 e = w16, o = w8;
        tL[0] = 0x3c00u; tL[1] = pkh(o);
#pragma unroll
        for (int j = 1; j < 8; ++j) { o = cmul(o, m16); tL[2 * j] = pkh(e); tL[2 * j + 1] = pkh(o); if (j < 7) e = cmul(e, m16); }
      }
      const f2 w1v = cexp(1.f);
      const cm m1 = mkcm(w1v), m128 = mkcm(cexp(128.f));
      f2 u = f2{bflo(pCr), bflo(pCi)} * asf((254u - ew) << 23), v = cmul(u, m128), bs = cmul(v, m128);
#pragma unroll
      for (int j = 0; j < 8; ++j) { t0[j] = pkh(u); t0[8 + j] = pkh(v); if (j < 7) { u = cmul(u, m1); v = cmul(v, m1); } }
#pragma unroll
      for (int j = 0; j < 16; ++j) { OPQ(tL[j]); OPQ(t0[j]); }
      STAMP(8);
      SETM0(km0);
      kput<16>(tL); frags(Bf);
      SB();
      const f2 w2v = cmul(w1v, m1);
      const cm m2 = mkcm(w2v), m4 = mkcm(cmul(w2v, m2));
      u = bs; v = cmul(bs, m128); bs = cmul(v, m128);
#pragma unroll
      for (int j = 0; j < 4; ++j) { t1[j] = pkh(u); t1[4 + j] = pkh(v); if (j < 3) { u = cmul(u, m2); v = cmul(v, m2); } }
#pragma unroll
      for (int j = 0; j < 8; ++j) OPQ(t1[j]);
      kput<16>(t0); frags(Af);
      SB();
#pragma unroll
      for (int j = 0; j < 4; ++j) { t1[8 + 2 * j] = pkh(bs); t1[9 + 2 * j] = pkh(cmul(bs, m4)); bs = cmul(bs, m128); }
#pragma unroll
      for (int j = 8; j < 16; ++j) OPQ(t1[j]);
      SB();
      kmm(0);
      STAMP(20);
      kput<16>(t1); frags(Af);
      SB();
#pragma unroll
      for (int j = 0; j < 9; ++j) { t2[j] = pkh(bs); if (j < 8) bs = cmul(bs, m128); }
#pragma unroll
      for (int j = 0; j < 9; ++j) OPQ(t2[j]);
      SB();
      kmm(1);
      kput<9>(t2); frags(Af);
    }
    STAMP(2);

    f16v acc[2];
#pragma unroll
    for (int i = 0; i < 16; ++i) { acc[0][i] = 0.f; acc[1][i] = 0.f; }
    float cs0 = 0.f, cs1 = 0.f;
    v4u stg;
    u64 tag;
    float A0, A1;
    unsigned tw[4], tr[9], pra;
    {
      v4u fB[2][4], fA[2][2][4];
      auto rdB = [&](int i, int k) { fB[i & 1][k] = *lp<v4u>(aB[slot_of(i) >= 98304][k] + ((slot_of(i) - 32768) & 32768)); };
      auto rdA = [&](int i, int m, int k) { fA[i & 1][m][k] = trfrag(aA[slot_of(i) >= 98304][m][k] + ((slot_of(i) - 32768) & 32768)); };
      auto mm = [&](int i, int m, int k) { acc[m] = mfma32(fA[i & 1][m][k], fB[i & 1][k], acc[m]); };
      auto csum = [&](int i, int j) {
        cs0 += bflo(fA[i & 1][0][0][j]); cs1 += bflo(fA[i & 1][1][0][j]); OPQ(cs0); OPQ(cs1);
        cs0 += bfhi(fA[i & 1][0][0][j]); cs1 += bfhi(fA[i & 1][1][0][j]); OPQ(cs0); OPQ(cs1);
      };
#pragma unroll
      for (int i = 0; i < 7; ++i) {
        if (i == 0) asm volatile("s_waitcnt vmcnt(4) lgkmcnt(0)\n\ts_barrier" : "+v"(Ev), "+v"(pb1a), "+v"(pb1b) :: "memory");
        else if (i == 5) asm volatile("s_waitcnt vmcnt(5)\n\ts_barrier" ::: "memory");
        else asm volatile("s_waitcnt vmcnt(4)\n\ts_barrier" ::: "memory");
        if (i == 0) STAMP(3);
        if (i == 1) STAMP(4);
        if (i == 3) STAMP(19);
        SB();
#pragma unroll
        for (int k = 0; k < 4; ++k) {
          if (i > 0) mm(i - 1, 0, k);
          SB();
          if (i == 4 && k == 0) ALD_B128S(stg, vst, p.ws, sst);
          if (i == 0 && k == 0) {
            kmm(2);
            unsigned pwa = w * 3072 + lane * 16; OPQ(pwa);
            *lp<f4>(pwa) = kc[0]; *lp<f4>(pwa + 1024) = kc[1];
            *lp<f2>(YSC + hg * 8) = f2{ysc, 0.f};
            *lp<f4>(pwa + 2048) = kc[2];
            SB();
          }
          if (i <= 4) { if (k == 0) SETM0(m0w + slot_of(i + 2)); dmaD(i + 2, k); } else dmaW(i - 5, k);
          SB();
          rdA(i, 0, k);
          if (i > 0) csum(i - 1, k);
          SB();
          if (i > 0) mm(i - 1, 1, k);
          SB();
          rdB(i, k); rdA(i, 1, k);
          SB();
        }
        if (i == 0) {
          pra = hg * 12288 + (tg < 3 ? tg : 2) * 1024 + lane * 16; OPQ(pra);
          tag = (((u64)(unsigned)__builtin_amdgcn_readfirstlane(Ev.y) << 32) | (unsigned)__builtin_amdgcn_readfirstlane(Ev.x)) + 1;
          const unsigned t3 = tg < 3 ? tg : 2, tb0 = TTAB + 1536 * hg;
          const unsigned shL = t3 == 0 ? 4u : t3 == 1 ? 3u : 1u, shH = t3 == 0 ? 4u : t3 == 1 ? 2u : 1u;
          const unsigned s1L = t3 < 2 ? 4u : 64u, s1H = t3 == 0 ? 4u : 64u, s0 = t3 == 2 ? 32u : 2u;
          tw[0] = tb0 + 512 * t3 + 256 * hf + (t3 == 0 ? 8u : 128u) * (q4 & 1) + (l15 << (hf ? shH : shL));
          tw[1] = tw[0] + s0; tw[2] = tw[0] + (hf ? s1H : s1L); tw[3] = tw[2] + s0;
          const unsigned g = 255 - (tid & 255);
          const unsigned rL = tg < 2 ? 3u : tg == 2 ? 2u : 1u, rH = tg < 2 ? 3u : tg == 2 ? 2u : 0u, rho = hf ? rH : rL;
          const unsigned oL = tg < 2 ? 0u : tg == 2 ? 0x02000000u : 0x04020000u, oH = tg == 3 ? 0x06040200u : oL;
          const unsigned pL = tg < 2 ? 0x02020200u : tg == 2 ? 0x04040202u : 0x08060404u, pH = tg == 3 ? 0x0e0c0a08u : pL;
          unsigned olo = hf ? oH : oL, ohi = (hf ? pH : pL) | ((2 * g) & (tg < 2 ? 2u : 0u));
          OPQ(olo); OPQ(ohi);
          const unsigned ta = tb0 + (rho << 8) + ((16 * g) >> rho);
#pragma unroll
          for (int e = 0; e < 8; ++e) tr[7 - e] = ta + (((e < 4 ? olo : ohi) >> (8 * (e & 3))) & 0xffu);
          tr[8] = g == 0 ? YSC + 4 : ta - (g <= 32 ? 2u : 0u);
#pragma unroll
          for (int e = 0; e < 4; ++e) OPQ(tw[e]);
#pragma unroll
          for (int e = 0; e < 9; ++e) OPQ(tr[e]);
          SB();
        }
      }
      STAMP(21);
      mm(6, 0, 0); SB(); csum(6, 0); csum(6, 1); SB(); mm(6, 1, 0); SB(); csum(6, 2); csum(6, 3);
      *lp<f2>(CSX + w * 512 + lane * 8) = f2{cs0, cs1};
      SB();
      mm(6, 0, 1); mm(6, 1, 1);
      SB();
      BAR();
      STAMP(5);
      unsigned ca = CSX + hg * 2048 + lane * 8; OPQ(ca);
      const f2 c0 = *lp<f2>(ca), c1 = *lp<f2>(ca + 512), c2 = *lp<f2>(ca + 1024), c3 = *lp<f2>(ca + 1536);
      SB();
      mm(6, 0, 2); mm(6, 0, 3); mm(6, 1, 2); mm(6, 1, 3);
      SB();
      const f2 c = (c0 + c1) + (c2 + c3);
      auto s0 = __builtin_amdgcn_permlane32_swap(asu(c.x), asu(c.x), false, false);
      auto s1 = __builtin_amdgcn_permlane32_swap(asu(c.y), asu(c.y), false, false);
      float t0 = asf(s0[0]) + asf(s0[1]), t1 = asf(s1[0]) + asf(s1[1]);
      asm volatile("" : "+v"(t0), "+v"(t1));
      A0 = hf ? bflo(pb1a) : t0; A1 = hf ? bflo(pb1b) : t1;
    }
    const v2u tg2 = {(unsigned)tag, (unsigned)(tag >> 32)};
    float av;
    {
      asm volatile("s_waitcnt vmcnt(8)" : "+v"(stg) :: "memory");
      if (__builtin_expect(__builtin_amdgcn_ballot_w64(stg.y != tg2.x || stg.w != tg2.x) != 0, 0)) {
#pragma unroll 1
        for (unsigned n = 0; n < SPIN_CAP; ++n) {
          asm volatile("buffer_load_dwordx4 %0, %1, %2, %3 offen sc1\n\ts_waitcnt vmcnt(0)" : "=&v"(stg) : "v"(vst), "s"(p.ws), "s"(sst) : "memory");
          if (__builtin_amdgcn_ballot_w64(stg.y != tg2.x || stg.w != tg2.x) == 0) break;
          __builtin_amdgcn_s_sleep(1);
        }
      }
      const float Bv = asf(hf ? stg.z : stg.x);
      av = __builtin_amdgcn_rcpf(asf(stg.z));
      acc[0] = __builtin_amdgcn_mfma_f32_32x32x2f32(A0, Bv, acc[0], 0, 0, 0);
      acc[1] = __builtin_amdgcn_mfma_f32_32x32x2f32(A1, Bv, acc[1], 0, 0, 0);
    }
    STAMP(6);
    aW[1][0] = aW[0][0] + 65536; aW[1][1] = aW[0][1] + 65536; OPQ(aW[1][0]); OPQ(aW[1][1]);
    unsigned vh = WS_H + ((4 * tt + tg) * 112 + 8 * sl + 4 * hg) * 1024 + lane * 16;
    f2 gc1 = {-0.10294324f, -0.10294324f}, gc2 = {-2.3022082f, -2.3022082f};
    OPQ(vh); OPQ(gc1); OPQ(gc2);
    auto gelu_q = [&](int qq, int wi) {
      v4u o;
#pragma unroll
      for (int j = 0; j < 4; ++j) {
        const int b = 8 * (qq & 1) + 2 * j;
        const f2 v = f2{acc[qq >> 1][b], acc[qq >> 1][b + 1]} * av;
        const f2 z = v * fma2(v * v, gc1, gc2);
        const f2 d = f2{__builtin_amdgcn_exp2f(z.x), __builtin_amdgcn_exp2f(z.y)} + 1.0f;
        unsigned r = pkbf(v * f2{__builtin_amdgcn_rcpf(d.x), __builtin_amdgcn_rcpf(d.y)});
        if (wi >= 0) { OPQ(r); if (j == 0) LGK0(); dmaW(wi, j); }
        o[j] = r;
      }
      st128(o, p.ws, vh + qq * 1024, 0, 16);
    };
    v2u xf;
    v4u uv;
    unsigned vf = (w * 4 + q4) * 128; OPQ(vf);
    ALD_B64S(xf, vf, p.ws, WS_FX);
    {
      const f4 p0 = *lp<f4>(pra), p1 = *lp<f4>(pra + 3072), p2 = *lp<f4>(pra + 6144), p3 = *lp<f4>(pra + 9216);
      *lp<unsigned>(UARR + (w >> 1) * UCP + min((w & 1) * 64 + lane, (w & 2) ? 111u : 112u) * 4) = 0;
      SB();
      gelu_q(0, 2);
      SB();
      const f4 v4 = (p0 + p1) + (p2 + p3);
      const unsigned h0 = pkh(v4.xy), h1 = pkh(v4.zw);
      *lp<u16>(tw[0]) = (u16)h0; *lp<u16>(tw[1]) = (u16)(h0 >> 16); *lp<u16>(tw[2]) = (u16)h1; *lp<u16>(tw[3]) = (u16)(h1 >> 16);
      SB();
    }
    gelu_q(1, -1);
    asm volatile("s_waitcnt vmcnt(6)" : "+v"(xf) :: "memory");
    await64(xf, p.ws, vf, WS_FX, tag);
    ALD_B128S(uv, tid * 16, p.ws, WS_XN + cp * 8192);
    BAR();
    {
      unsigned tv[9];
#pragma unroll
      for (int e = 0; e < 9; ++e) tv[e] = *lp<u16>(tr[e]);
      *lp<unsigned>(q4 * KCP + 4096 + l15 * 4) = 0;
      readW(0);
      SB();
      gelu_q(2, -1);
      SB();
      unsigned r0 = tv[0] | tv[1] << 16, r1 = tv[2] | tv[3] << 16, r2 = tv[4] | tv[5] << 16, r3 = tv[6] | tv[7] << 16;
      OPQ(r0); OPQ(r1); OPQ(r2); OPQ(r3);
      unsigned kb = hg * 2 * KCP + (tid & 255) * 16; OPQ(kb);
      *lp<v4u>(kb + KCP) = v4u{r0, r1, r2, r3};
      *lp<v4u>(kb) = v4u{__builtin_amdgcn_alignbit(r1, r0, 16), __builtin_amdgcn_alignbit(r2, r1, 16), __builtin_amdgcn_alignbit(r3, r2, 16), __builtin_amdgcn_alignbit(tv[8], r3, 16)};
      readW(1);
      SB();
    }
    gelu_q(3, -1);
    STAMP(7);
    {
      asm volatile("s_waitcnt vmcnt(2)" : "+v"(uv) :: "memory");
      const unsigned a0 = __builtin_amdgcn_perm(uv.y, uv.x, 0x05040100), a1 = __builtin_amdgcn_perm(uv.w, uv.z, 0x05040100);
      const unsigned c0 = __builtin_amdgcn_perm(uv.y, uv.x, 0x07060302), c1 = __builtin_amdgcn_perm(uv.w, uv.z, 0x07060302);
      unsigned ub = UARR + 448 + tid * 8, ub2 = ub + 2 * UCP; OPQ(ub); OPQ(ub2);
      *lp<unsigned>(ub + 4) = a0; *lp<unsigned>(ub + 8) = a1;
      *lp<v2u>(ub + UCP) = v2u{a0, a1};
      *lp<unsigned>(ub2 + 4) = c0; *lp<unsigned>(ub2 + 8) = c1;
      *lp<v2u>(ub2 + UCP) = v2u{c0, c1};
      readW(2);
    }
    v4u H[14];
    unsigned sh0 = WS_H + (2 * tb * 112 + 14 * w) * 1024; OPQS(sh0);
    unsigned vl = lane * 16; OPQ(vl);
    auto loadH = [&](int half, int k) { return ld128(p.ws, vl + (k & 3) * 1024, sh0 + half * 112 * 1024 + (k >> 2) * 4096, 16); };
    {
      const unsigned jh = w & 1, thh = (w >> 1) & 1;
      f4 ya[4];
#pragma unroll
      for (int i = 0; i < 4; ++i) ya[i] = f4{0.f, 0.f, 0.f, 0.f};
      unsigned fa = (hg * 2 + (l15 & 1)) * KCP + 2 * (2044 - 1792 - 96 - 128 * jh - 4 * (l15 >> 1) + 8 * q4);
      unsigned ga = UARR + (hg * 2 + (l15 & 1)) * UCP + 2 * (224 - 96 - 128 * jh + 32 * (l15 >> 1) + 8 * q4);
      unsigned fb = fa + 8;
      unsigned vq = hf * 4096 + min(((14 * w) >> 2) + ((lane >> 2) & 7), (14 * w + 13) >> 2) * 128;
      OPQ(fa); OPQ(fb); OPQ(ga); OPQ(vq);
      unsigned dskp;
      v2u hfl;
      STAMP(17);
      BAR();
      STAMP(9);
      auto hook = [&](auto M) {
        constexpr int m = M.v, r = m - HST;
        if constexpr (m < 24 && m % 2 == 0) dmaW(3 + m / 8, (m / 2) & 3);
        if constexpr (m == 5) { if (hg) { VMW(3); if (lane == 0) st64(tg2, p.ws, 0, WS_FH + (4 * tt + tg) * 4096 + (2 * sl + hg) * 128, 16); } }
        if constexpr (m == 15) { if (!hg) { VMW(8); if (lane == 0) st64(tg2, p.ws, 0, WS_FH + (4 * tt + tg) * 4096 + (2 * sl + hg) * 128, 16); } }
        if constexpr (m == 23) { VMW(8); readW(3); }
        if constexpr (m == 27) asm volatile("s_waitcnt lgkmcnt(4)" ::: "memory");
        if constexpr (m >= 27 && m < 35 && m % 2 == 1) dmaW(6, ((m - 27) / 2) & 3);
        if constexpr (m == HST - 8) { dskp = ld32(p.Dsk, 0, cp * 4, 0); hfl = ld64(p.ws, vq, WS_FH + tb * 8192, 16); }
        if constexpr (r == 0) { VMW(0); await64(hfl, p.ws, vq, WS_FH + tb * 8192, tag); STAMP(18); }
        if constexpr (r >= 0 && r % 2 == 0 && r / 2 < 14) H[r / 2] = loadH(0, r / 2);
        if constexpr (r >= 0 && r % 2 == 1 && r / 2 < 12) readW1(4 + r / 8, (r / 2) & 3);
      };
      if (thh) conv_all<1>(fa, fb, ga, ya, hook); else conv_all<0>(fa, fb, ga, ya, hook);
      STAMP(10);
      unsigned y0a = YBUF + w * 4096 + 4 * (32 * (l15 >> 1) + ((2 * (l15 & 1) + 8 * q4) ^ (4 * (l15 >> 1)))), y0b = y0a ^ 16;
      OPQ(y0a); OPQ(y0b);
#pragma unroll
      for (int i = 0; i < 4; ++i) { *lp<f2>(y0a + i * 1024) = ya[i].xy; *lp<f2>(y0b + i * 1024) = ya[i].zw; SB(); }
      unsigned yr = YBUF + (((w + 1) >> 1) & 1) * 8192 + (w >> 1) * 1024 + ((lane ^ (lane >> 3)) << 4); OPQ(yr);
      BAR();
      STAMP(11);
      const f2 sc2 = (*lp<f4>(YSC)).xz;
      const f4 y0 = *lp<f4>(yr) + *lp<f4>(yr + 4096), y1 = *lp<f4>(yr + 16384) + *lp<f4>(yr + 20480);
      const f2 D2 = bf2(dskp);
      v4u sv;
#pragma unroll
      for (int i = 0; i < 4; ++i) sv[i] = pkbf(fma2(f2{y0[i], y1[i]}, sc2, h2f(uv[i]) * D2));
      st128(sv, p.ws, WS_S + cp * 8192 + tid * 16, 0, 16);
      BAR();
    }
    STAMP(12);
    const unsigned etok = tb * 64 + (tid >> 3), ech = cb * 64 + (tid & 7) * 8;
    unsigned pw[8];
    v2u sfl;
    v4u xe, b2e;
    v4u sq;
    auto pubS = [&] { if (lane == 0) st64(tg2, p.ws, 0, WS_FS + ((cp >> 5) * 8 + w) * 4096 + (cp & 31) * 128, 16); };
    if (hg) { VMW(0); pubS(); }
    SB();
#pragma unroll
    for (int half = 0; half < 2; ++half) {
      f16v o0, o1;
#pragma unroll
      for (int i = 0; i < 16; ++i) { o0[i] = 0.f; o1[i] = 0.f; }
#pragma unroll
      for (int k = 0; k < 14; ++k) {
        o0 = mfma32(W[k >> 1][2 * (k & 1)], H[k], o0);
        o1 = mfma32(W[k >> 1][2 * (k & 1) + 1], H[k], o1);
        if (half == 0) H[k] = loadH(1, k);
        SB();
        if (half == 0 && k == 12) {
          if (hg == 0) { VMW(13); pubS(); }
          SB();
        }
        if (half == 0 && k == 10) {
          const unsigned pb = w * 16384 + l31 * 256 + (((hf * 8) ^ (l31 & 7) ^ ((l31 & 2) << 2)) << 4);
#pragma unroll
          for (int g = 0; g < 8; ++g) { pw[g] = pb ^ (g << 4); OPQ(pw[g]); }
          SB();
        }
        if (half == 1 && k == 1) { sfl = ld64(p.ws, vf, WS_FS + (cb * 8 + (tb >> 2)) * 4096, 16); SB(); }
      }
      if (half == 0) {
        xe = ld128(p.x, etok * 896 + ech * 2, 0, 0);
        b2e = ld128(p.b2, ech * 2, 0, 0);
        STAMP(13);
      } else {
        STAMP(14);
        await64(sfl, p.ws, vf, WS_FS + (cb * 8 + (tb >> 2)) * 4096, tag);
        STAMP(22);
        sq = ld128(p.ws, (tid >> 4) * 8192 + l15 * 16, WS_S + cb * 32 * 8192 + tb * 256, 16);
      }
#pragma unroll
      for (int g = 0; g < 4; ++g) {
        *lp<f4>(pw[g] + half * 8192) = f4{o0[4 * g], o0[4 * g + 1], o0[4 * g + 2], o0[4 * g + 3]};
        *lp<f4>(pw[4 + g] + half * 8192) = f4{o1[4 * g], o1[4 * g + 1], o1[4 * g + 2], o1[4 * g + 3]};
      }
      SB();
    }
    *lp<v4u>(SBUF + (tid >> 4) * 256 + ((l15 ^ w) << 4)) = sq;
    STAMP(23);
    {
      const unsigned row = tid >> 3;
      unsigned sr = SBUF + (tid & 7) * 1024 + (((row >> 2) ^ (tid & 7)) << 4) + (row & 3) * 4; OPQ(sr);
      unsigned pa0 = row * 256 + (((tid & 7) ^ (row & 7) ^ ((row & 2) << 2)) << 4), pb0 = pa0 ^ 128, pa1 = pa0 + 65536, pb1 = pb0 + 65536;
      OPQ(pa0); OPQ(pb0); OPQ(pa1); OPQ(pb1);
      BAR();
      STAMP(15);
      f4 a[8], b[8];
#pragma unroll
      for (int k = 0; k < 8; ++k) { a[k] = *lp<f4>((k < 4 ? pa0 : pa1) + (k & 3) * 16384); b[k] = *lp<f4>((k < 4 ? pb0 : pb1) + (k & 3) * 16384); }
      unsigned se[4];
#pragma unroll
      for (int i = 0; i < 4; ++i) se[i] = *lp<unsigned>(sr + i * 256);
      SB();
      const f4 sa = ((a[0] + a[1]) + (a[2] + a[3])) + ((a[4] + a[5]) + (a[6] + a[7]));
      const f4 sb = ((b[0] + b[1]) + (b[2] + b[3])) + ((b[4] + b[5]) + (b[6] + b[7]));
      v4u ov;
#pragma unroll
      for (int i = 0; i < 4; ++i) {
        const f2 o = (i == 0 ? sa.xy : i == 1 ? sa.zw : i == 2 ? sb.xy : sb.zw) + bf2(b2e[i]);
        const f2 z = o * -1.4426950408889634f;
        const f2 d = f2{__builtin_amdgcn_exp2f(z.x), __builtin_amdgcn_exp2f(z.y)} + 1.0f;
        const f2 g = o * f2{__builtin_amdgcn_rcpf(d.x), __builtin_amdgcn_rcpf(d.y)};
        ov[i] = pkbf(fma2(g, bf2(se[i]), bf2(xe[i])));
      }
      st128(ov, p.out, etok * 896 + ech * 2, 0, 16);
    }
    STAMP(16);
  }
}

extern "C" void kernel_launch(void* const* d_in, const int* in_sizes, int n_in, void* d_out, int out_size, void* d_ws, size_t ws_size,
                              hipStream_t stream) {
  const unsigned FL = 0x00020000u;
  HostParams h = {{{d_in[1], DM * 512, FL}, {d_in[2], DM * 512, FL}, {d_in[3], DM * 512, FL}, {d_in[4], DM * 512, FL},
                   {d_ws, WS_END, FL}, {d_in[0], LT * DM * 2, FL}, {d_in[8], DM * FF * 2 + 4096, FL}, {d_in[9], FF * 2, FL},
                   {d_in[5], DM * 2, FL}, {d_in[10], FF * DM * 2, FL}, {d_in[6], DM * 2, FL}, {d_in[11], DM * 2, FL},
                   {d_out, LT * DM * 2, FL}}};
  static_assert(sizeof(HostParams) == sizeof(Params), "descriptor block layout");
  Params p;
  __builtin_memcpy(&p, &h, sizeof(p));
  s4d_mlp_gate_kernel<<<256, 512, 0, stream>>>(p);
}
```
